```python
import torch

def _softmax(x, axis=-1):
    return torch.softmax(x, dim=axis)

def _gelu(x):
    return torch.nn.functional.gelu(x, approximate='tanh')

def _silu(x):
    return torch.nn.functional.silu(x)

def _sigmoid(x):
    return torch.sigmoid(x)

def _layernorm(x, axis=-1, eps=1e-05):
    m = x.mean(dim=axis, keepdim=True)
    v = x.var(dim=axis, keepdim=True, unbiased=False)
    return (x - m) / torch.sqrt(v + eps)

def _scatter_add(tgt, idx, src, axis=0):
    return tgt.clone().index_add_(axis, idx, src)

def _segment_sum(src, seg, n):
    out = torch.zeros((n,) + src.shape[1:], dtype=src.dtype)
    return out.index_add_(0, seg, src)

def _pad(x, pw):
    flat = tuple((v for pair in reversed(pw) for v in pair))
    return torch.nn.functional.pad(x, flat)
kernel_name = 'adaLN_cond__memory_aug_recurrent_mem_tr__glu_gated_swiglu__prompt'

def setup_inputs():

    _w = lambda a: (lambda t: t.to(torch.bfloat16) if t.is_floating_point() else t)(torch.as_tensor(a))

    def _A_setup():
        B, S, Dm = (1, 2048, 640)
        H, Dh = (32, 128)
        M, seg = (8, 32)
        seg = min(seg, S)
        S = S // seg * seg
        return {'x': _w((lambda *s: torch.randn(*s, dtype=torch.bfloat16) * 0.05)(B, S, Dm)), 'mem0': _w((lambda *s: torch.randn(*s, dtype=torch.bfloat16) * 0.8)(B, M, Dm)), 'Wq': _w((lambda *s: torch.randn(*s, dtype=torch.bfloat16) * 0.8)(Dm, H, Dh) * Dm ** (-0.5)), 'Wk': _w((lambda *s: torch.randn(*s, dtype=torch.bfloat16) * 0.8)(Dm, H, Dh) * Dm ** (-0.5)), 'Wv': _w((lambda *s: torch.randn(*s, dtype=torch.bfloat16) * 0.8)(Dm, H, Dh) * Dm ** (-0.5)), 'Wo': _w((lambda *s: torch.randn(*s, dtype=torch.bfloat16) * 0.8)(H, Dh, Dm) * (H * Dh) ** (-0.5)), 'seg_len': seg}
    _A_in = _A_setup()

    def _B_setup():
        B, S, D, F = (1, 2048, 640, 1728)
        return {'x': _w((lambda *s: torch.randn(*s, dtype=torch.bfloat16) * 0.05)(B, S, D)), 'w1': _w((lambda *s: torch.randn(*s, dtype=torch.bfloat16) * 0.4)(D, F) * (2.0 / D) ** 0.5), 'w3': _w((lambda *s: torch.randn(*s, dtype=torch.bfloat16) * 0.4)(D, F) * (2.0 / D) ** 0.5), 'w2': _w((lambda *s: torch.randn(*s, dtype=torch.bfloat16) * 0.4)(F, D) * (2.0 / F) ** 0.5)}
    _B_in = _B_setup()
    return {'A_x': _A_in['x'], 'A_mem0': _A_in['mem0'], 'A_Wq': _A_in['Wq'], 'A_Wk': _A_in['Wk'], 'A_Wv': _A_in['Wv'], 'A_Wo': _A_in['Wo'], 'A_seg_len': _A_in['seg_len'], 'B_x': _B_in['x'], 'B_w1': _B_in['w1'], 'B_w3': _B_in['w3'], 'B_w2': _B_in['w2'], 'c': _w((lambda *s: torch.randn(*s, dtype=torch.bfloat16) * 0.1)(1, 640) * 8.0), 'w_mod': _w((lambda *s: torch.randn(*s, dtype=torch.bfloat16) * 0.1)(640, 6 * 640) * 640 ** (-0.5) * 8.0)}

def reference(A_x, A_mem0, A_Wq, A_Wk, A_Wv, A_Wo, A_seg_len, B_x, B_w1, B_w3, B_w2, c, w_mod):

    def _A_ref(x, mem0, Wq, Wk, Wv, Wo, seg_len):
        B, S, Dm = x.shape
        H, Dh = (Wq.shape[1], Wq.shape[2])
        M = mem0.shape[1]
        scale = Dh ** (-0.5)
        n_seg = S // seg_len
        L = M + seg_len
        idx = torch.arange(L)
        mask = (idx[:, None] < M) | (idx[:, None] >= idx[None, :])
        mem = mem0
        outs = []
        for s in range(n_seg):
            xs = x[:, s * seg_len:(s + 1) * seg_len, :].clone()
            h = torch.concatenate([mem, xs], axis=1)
            q = torch.einsum('btd,dhe->bhte', h, Wq)
            k = torch.einsum('btd,dhe->bhte', h, Wk)
            v = torch.einsum('btd,dhe->bhte', h, Wv)
            scores = torch.einsum('bhqe,bhke->bhqk', q, k) * scale
            scores = torch.where(mask[None, None], scores, -1000000000.0)
            p = _softmax(scores, axis=-1)
            o = torch.einsum('bhqk,bhke->bhqe', p, v)
            o = torch.einsum('bhte,hed->btd', o, Wo)
            mem = o[:, :M, :].clone()
            outs.append(o[:, M:, :])
        return torch.concatenate(outs, axis=1)

    def _B_ref(x, w1, w3, w2):
        gate = torch.einsum('bsd,df->bsf', x, w1)
        up = torch.einsum('bsd,df->bsf', x, w3)
        h = _silu(gate) * up
        out = torch.einsum('bsf,fd->bsd', h, w2)
        return out
    x = A_x
    mod = (_silu(c) @ w_mod).reshape(c.shape[0], 6, 1, -1)
    sa, ca, ga, sb, cb, gb = (mod[:, 0].clone(), mod[:, 1].clone(), mod[:, 2].clone(), mod[:, 3].clone(), mod[:, 4].clone(), mod[:, 5].clone())
    h = _layernorm(x, axis=-1) * (1 + ca) + sa
    x = x + ga * _A_ref(h, A_mem0, A_Wq, A_Wk, A_Wv, A_Wo, A_seg_len)
    h = _layernorm(x, axis=-1) * (1 + cb) + sb
    x = x + gb * _B_ref(h, B_w1, B_w3, B_w2)
    return x
```

```cpp
#include <hip/hip_runtime.h>
#include <hip/hip_bf16.h>

#define LAS __attribute__((address_space(3)))
#define GAS __attribute__((address_space(1)))
#define DEVI __device__ __forceinline__
typedef unsigned short u16;
typedef __bf16 bf16x4 __attribute__((ext_vector_type(4)));
typedef __bf16 bf16x8 __attribute__((ext_vector_type(8)));
typedef __bf16 bf16x16 __attribute__((ext_vector_type(16)));
typedef float f32x2 __attribute__((ext_vector_type(2)));
typedef float f32x4 __attribute__((ext_vector_type(4)));
typedef float f32x16 __attribute__((ext_vector_type(16)));
typedef unsigned v2u __attribute__((ext_vector_type(2)));
typedef unsigned v4u __attribute__((ext_vector_type(4)));
typedef unsigned v8u __attribute__((ext_vector_type(8)));
typedef __bf16 bf16x2 __attribute__((ext_vector_type(2)));
typedef LAS unsigned char lds8;
#define RLX_AGENT __ATOMIC_RELAXED, __HIP_MEMORY_SCOPE_AGENT

constexpr int DM = 640, NH = 32, NSEG = 64, FF = 1728;
constexpr unsigned MB = 1u << 20;
constexpr unsigned CTL_BYTES = 16384;
constexpr unsigned C_READY = 0, C_ABORT = 128, C_SEL = 256, C_FK = 1024, C_FU = 8192 - 2048 - 2048 + 4096 - 3072;
constexpr unsigned OFF_MOD = 65536;
constexpr unsigned OFF_RINGLO = 131072;
constexpr unsigned OFF_HF = 5 * MB, OFF_KX = 8 * MB, OFF_QX = 24 * MB, OFF_KM = 40 * MB, OFF_YT = 44 * MB, OFF_UT = 64 * MB;
constexpr unsigned OFF_PART = 144 * MB, OFF_X1 = 164 * MB, OFF_H2F = 170 * MB, OFF_GF = 176 * MB, WS_USED = 184 * MB;
static_assert(C_FU == 5120 && C_FU + 64 * 128 <= CTL_BYTES, "ctl map");
constexpr unsigned RING_SLOT = 40960, RING_M = 0, RING_P = 16384;
constexpr unsigned SENT = 0xFFFFFFFFu;
constexpr unsigned OOB = 0xFFFFFF00u;
constexpr int NSYNC = 104;
constexpr unsigned SPIN_CAP = 400000u;

struct Params {
    const u16 *x, *mem0, *wq, *wk, *wv, *wo, *w1, *w3, *w2, *c, *wmod;
    unsigned char* ws;
    u16* out;
};

DEVI float bf2f(unsigned h) { return __uint_as_float(h << 16); }
DEVI float bflo(unsigned u) { return __uint_as_float(u << 16); }
DEVI float bfhi(unsigned u) { return __uint_as_float(u & 0xffff0000u); }
DEVI unsigned pk2(float a, float b) { bf16x2 r; r[0] = (__bf16)a; r[1] = (__bf16)b; return __builtin_bit_cast(unsigned, r); }
DEVI float rbf16(float a) { return (float)(__bf16)a; }
DEVI unsigned safe(unsigned u) { return u < 0xFFFFFFFEu ? u : 0xFFFFFFFEu; }
DEVI __amdgpu_buffer_rsrc_t mkrs(const void* p, unsigned bytes) { return __builtin_amdgcn_make_buffer_rsrc((void*)p, 0, bytes, 0x00020000); }
DEVI v4u ld_sc1(__amdgpu_buffer_rsrc_t r, unsigned off) { return __builtin_amdgcn_raw_buffer_load_b128(r, off, 0, 16); }
DEVI void st_sc1(__amdgpu_buffer_rsrc_t r, unsigned off, v4u v) { __builtin_amdgcn_raw_buffer_store_b128(v, r, off, 0, 16); }
DEVI v4u ld_pl(__amdgpu_buffer_rsrc_t r, unsigned off) { return __builtin_amdgcn_raw_buffer_load_b128(r, off, 0, 0); }
DEVI void st_pl(__amdgpu_buffer_rsrc_t r, unsigned off, v4u v) { __builtin_amdgcn_raw_buffer_store_b128(v, r, off, 0, 0); }
DEVI v4u gld16(const void* p) { return *(const GAS v4u*)p; }
DEVI v4u lds_rd16(lds8* p) { return *(LAS v4u*)p; }
DEVI void lds_wr16(lds8* p, v4u v) { *(LAS v4u*)p = v; }
DEVI void lds_wr8(lds8* p, v2u v) { *(LAS v2u*)p = v; }
DEVI v2u lds_tr(lds8* p) { return __builtin_bit_cast(v2u, __builtin_amdgcn_ds_read_tr16_b64_v4bf16((LAS bf16x4*)p)); }
DEVI bf16x8 as8(v4u v) { return __builtin_bit_cast(bf16x8, v); }
DEVI bf16x8 cat8(v2u a, v2u b) { v4u v = {a.x, a.y, b.x, b.y}; return __builtin_bit_cast(bf16x8, v); }
DEVI bf16x16 cat16(v2u a, v2u b, v2u c, v2u d) { v8u v = {a.x, a.y, b.x, b.y, c.x, c.y, d.x, d.y}; return __builtin_bit_cast(bf16x16, v); }
DEVI f32x4 mfma16(bf16x8 a, bf16x8 b, f32x4 c) { return __builtin_amdgcn_mfma_f32_16x16x32_bf16(a, b, c, 0, 0, 0); }
DEVI f32x16 mfma32(bf16x8 a, bf16x8 b, f32x16 c) { return __builtin_amdgcn_mfma_f32_32x32x16_bf16(a, b, c, 0, 0, 0); }
DEVI f32x4 smf(bf16x8 a, bf16x16 b, f32x4 c, int idx) { return __builtin_amdgcn_smfmac_f32_16x16x64_bf16(a, b, c, idx, 0, 0); }
DEVI unsigned umax3(unsigned a, unsigned b, unsigned c) { return max(max(a, b), c); }
DEVI unsigned umax4(v4u v) { return max(max(v.x, v.y), max(v.z, v.w)); }
DEVI void cbar() { asm volatile("" ::: "memory"); }
DEVI void drain() { asm volatile("s_waitcnt vmcnt(0)" ::: "memory"); }
DEVI void sw32(unsigned a, unsigned b, unsigned& o0, unsigned& o1) { auto r = __builtin_amdgcn_permlane32_swap(a, b, false, false); o0 = r[0]; o1 = r[1]; }
DEVI void sw16(unsigned a, unsigned b, unsigned& o0, unsigned& o1) { auto r = __builtin_amdgcn_permlane16_swap(a, b, false, false); o0 = r[0]; o1 = r[1]; }
DEVI v4u sw16x2(unsigned a0, unsigned b0, unsigned a1, unsigned b1) { unsigned x, z, y, w; sw16(a0, b0, x, z); sw16(a1, b1, y, w); return (v4u){x, y, z, w}; }
DEVI float xmax_g(float v) {
    unsigned a, b; sw32(__float_as_uint(v), __float_as_uint(v), a, b); v = fmaxf(__uint_as_float(a), __uint_as_float(b));
    sw16(__float_as_uint(v), __float_as_uint(v), a, b); return fmaxf(__uint_as_float(a), __uint_as_float(b));
}
DEVI float xsum_g(float v) {
    unsigned a, b; sw32(__float_as_uint(v), __float_as_uint(v), a, b); v = __uint_as_float(a) + __uint_as_float(b);
    sw16(__float_as_uint(v), __float_as_uint(v), a, b); return __uint_as_float(a) + __uint_as_float(b);
}
DEVI f32x4 fold2(f32x4 a, f32x4 b) {
    f32x4 r;
#pragma unroll
    for (int i = 0; i < 4; ++i) { unsigned x, y; sw32(__float_as_uint(a[i]), __float_as_uint(b[i]), x, y); r[i] = __uint_as_float(x) + __uint_as_float(y); }
    return r;
}
DEVI unsigned physo(unsigned lo, unsigned selbit) { return ((lo >> 13) << 14) | selbit | (lo & 8191u); }
DEVI unsigned ctl_ld(unsigned char* ws, unsigned off) { return __hip_atomic_load((GAS unsigned*)(ws + off), RLX_AGENT); }
DEVI void ctl_st(unsigned char* ws, unsigned off, unsigned v) { __hip_atomic_store((GAS unsigned*)(ws + off), v, RLX_AGENT); }
DEVI bool spin_fail(unsigned& spins, unsigned char* ws) {
    ++spins;
    if ((spins & 1023u) == 0u) {
        if (spins > SPIN_CAP) { ctl_st(ws, C_ABORT, 1u); return true; }
        if (__builtin_amdgcn_readfirstlane(ctl_ld(ws, C_ABORT))) return true;
    }
    return false;
}
DEVI bool wait_ge(unsigned char* ws, unsigned off, unsigned want) {
    for (unsigned spins = 0;;) {
        unsigned v = ctl_ld(ws, off);
        if (__all(v >= want)) return true;
        __builtin_amdgcn_s_sleep(2);
        if (spin_fail(spins, ws)) return false;
    }
}

template <int NR>
DEVI void stage64(const unsigned char* src  , size_t ld, lds8* dst, int lane) {
    constexpr int NI = NR / 16;
    v4u v[NI];
#pragma unroll
    for (int it = 0; it < NI; ++it) v[it] = gld16(src + (size_t)it * 16 * ld);
    const int r = lane >> 2, pc = lane & 3;
    lds8* d = dst + r * 64 + ((pc ^ (((r >> 3) & 1) << 1)) * 16);
#pragma unroll
    for (int it = 0; it < NI; ++it) lds_wr16(d + it * 1024, v[it]);
}
DEVI void stage320(const unsigned char* src, lds8* dst, int lane) {
    v4u v[40];
#pragma unroll
    for (int it = 0; it < 40; ++it) { const int ci = it * 64 + lane, r = ci / 20, ch = ci % 20; v[it] = gld16(src + (size_t)r * 1280 + ch * 16); }
#pragma unroll
    for (int it = 0; it < 40; ++it) { const int ci = it * 64 + lane, r = ci / 20, ch = ci % 20; lds_wr16(dst + r * 320 + ((ch ^ (((r >> 3) & 1) << 1)) * 16), v[it]); }
}
DEVI void lds_fence() { asm volatile("s_waitcnt lgkmcnt(0)" ::: "memory"); }

__global__ void __launch_bounds__(256) k_mod(Params p) {
    __shared__ float sc[DM];
    __shared__ float red[32][65];
    const int tid = threadIdx.x;
    for (int i = tid; i < DM; i += 256) { float x = bf2f(p.c[i]); sc[i] = rbf16(x / (1.f + __expf(-x))); }
    __syncthreads();
    const int cg = tid & 7, ksl = tid >> 3, n0 = blockIdx.x * 64 + cg * 8;
    float acc[8] = {0, 0, 0, 0, 0, 0, 0, 0};
    v4u v[20];
#pragma unroll
    for (int i = 0; i < 20; ++i) v[i] = gld16(p.wmod + (size_t)(ksl * 20 + i) * 3840 + n0);
#pragma unroll
    for (int i = 0; i < 20; ++i) {
        const float s = sc[ksl * 20 + i];
#pragma unroll
        for (int j = 0; j < 4; ++j) { acc[2 * j] += s * bflo(v[i][j]); acc[2 * j + 1] += s * bfhi(v[i][j]); }
    }
#pragma unroll
    for (int j = 0; j < 8; ++j) red[ksl][cg * 8 + j] = acc[j];
    __syncthreads();
    if (tid < 64) { float t = 0; for (int k = 0; k < 32; ++k) t += red[k][tid]; ((float*)(p.ws + OFF_MOD))[blockIdx.x * 64 + tid] = rbf16(t); }
}

DEVI void ln_mod_store(float (&xv)[40], const float* mod_shift, const float* mod_scale, unsigned char* dstF, int row, int i16) {
    float s = 0;
#pragma unroll
    for (int j = 0; j < 40; ++j) s += xv[j];
#pragma unroll
    for (int o = 8; o >= 1; o >>= 1) s += __shfl_xor(s, o);
    const float mean = s * (1.f / DM);
    float q = 0;
#pragma unroll
    for (int j = 0; j < 40; ++j) { const float d = xv[j] - mean; q += d * d; }
#pragma unroll
    for (int o = 8; o >= 1; o >>= 1) q += __shfl_xor(q, o);
    const float rstd = 1.f / sqrtf(q * (1.f / DM) + 1e-5f);
    const int seg = row >> 5, tok = row & 31;
#pragma unroll
    for (int c = 0; c < 5; ++c) {
        const int ch = i16 + 16 * c;
        const f32x4 s0 = *(const f32x4*)(mod_shift + ch * 8), s1 = *(const f32x4*)(mod_shift + ch * 8 + 4);
        const f32x4 c0 = *(const f32x4*)(mod_scale + ch * 8), c1 = *(const f32x4*)(mod_scale + ch * 8 + 4);
        float h[8];
#pragma unroll
        for (int j = 0; j < 8; ++j) { const float sh = j < 4 ? s0[j & 3] : s1[j & 3], scv = j < 4 ? c0[j & 3] : c1[j & 3]; h[j] = (xv[c * 8 + j] - mean) * rstd * rbf16(1.f + scv) + sh; }
        v4u o = {pk2(h[0], h[1]), pk2(h[2], h[3]), pk2(h[4], h[5]), pk2(h[6], h[7])};
        *(GAS v4u*)(dstF + (size_t)seg * 40960 + ch * 512 + tok * 16) = o;
    }
}

__global__ void __launch_bounds__(256) k_ln1(Params p) {
    const int tid = threadIdx.x, row = blockIdx.x * 16 + (tid >> 4), i16 = tid & 15;
    float xv[40];
#pragma unroll
    for (int c = 0; c < 5; ++c) { const v4u v = gld16(p.x + (size_t)row * DM + (i16 + 16 * c) * 8);
#pragma unroll
        for (int j = 0; j < 4; ++j) { xv[c * 8 + 2 * j] = bflo(v[j]); xv[c * 8 + 2 * j + 1] = bfhi(v[j]); } }
    const float* mod = (const float*)(p.ws + OFF_MOD);
    ln_mod_store(xv, mod, mod + DM, p.ws + OFF_HF, row, i16);
}

constexpr int L_M = 0;
constexpr int L_T = 10240;
constexpr int L_RED = 0;
constexpr int LDS_MAIN = 160 * 1024;

DEVI bool gather_m(const Params& p, __amdgpu_buffer_rsrc_t ring, unsigned selbit, int s, lds8* L, int tid) {
    if (s == 0) {
        for (int ci = tid; ci < 640; ci += 256) {
            const int v = ci >> 4, hf = (ci >> 3) & 1, r = ci & 7;
            const GAS unsigned* src = (const GAS unsigned*)(p.mem0 + r * DM + 16 * v + 2 * hf);
            v4u o = {src[0], src[2], src[4], src[6]};
            lds_wr16(L + L_M + ci * 16, o);
        }
        return true;
    }
    const unsigned base = (unsigned)(s & 3) * RING_SLOT + RING_M;
    const unsigned o0 = physo(base + tid * 16, selbit), o1 = physo(base + 4096 + tid * 16, selbit), o2 = tid < 128 ? physo(base + 8192 + tid * 16, selbit) : OOB;
    for (unsigned spins = 0;;) {
        cbar();
        const v4u a = ld_sc1(ring, o0), b = ld_sc1(ring, o1), c = ld_sc1(ring, o2);
        const unsigned mx = umax3(umax4(a), umax4(b), umax4(c));
        if (__all(mx != SENT)) {
            lds_wr16(L + L_M + tid * 16, a); lds_wr16(L + L_M + 4096 + tid * 16, b);
            if (tid < 128) lds_wr16(L + L_M + 8192 + tid * 16, c);
            return true;
        }
        if (spin_fail(spins, p.ws)) return false;
    }
}

DEVI bool start_sync(const Params& p, int tid, unsigned& selbit) {
    drain();
    __syncthreads();
    if (tid == 0) __hip_atomic_fetch_add((GAS unsigned*)(p.ws + C_READY), 1u, RLX_AGENT);
    return true;
}
DEVI bool finish_sync(const Params& p, unsigned& selbit) {
    if (!wait_ge(p.ws, C_READY, NSYNC)) return false;
    if (!wait_ge(p.ws, C_SEL, 1u)) return false;
    selbit = (__builtin_amdgcn_readfirstlane(ctl_ld(p.ws, C_SEL)) - 1u) << 13;
    return true;
}

DEVI bf16x16 frag16_64(lds8* base_t, int ks) {
    return cat16(lds_tr(base_t + ks * 4096), lds_tr(base_t + ks * 4096 + 256), lds_tr(base_t + ks * 4096 + 2048), lds_tr(base_t + ks * 4096 + 2048 + 256));
}

DEVI void role_S(const Params& p, lds8* L, int h, __amdgpu_buffer_rsrc_t ring, __amdgpu_buffer_rsrc_t wsr) {
    const int tid = threadIdx.x, lane = tid & 63, wave = __builtin_amdgcn_readfirstlane(tid >> 6);
    const int g = lane >> 4, c = lane & 15;
    if (tid < 160) { const int sl = tid / 80, par = (tid / 40) & 1, i = tid % 40; v4u s4 = {SENT, SENT, SENT, SENT};
        st_sc1(ring, physo(sl * RING_SLOT + RING_P + h * 640 + i * 16, (unsigned)par << 13), s4); }
    unsigned selbit = 0;
    start_sync(p, tid, selbit);
    if (h == 0 && wave == 3) {
        unsigned long long t0, t1, t2; unsigned best0 = ~0u, best1 = ~0u;
        const unsigned pr = 4 * 2 * RING_SLOT + (lane & 7) * 16;
        for (int rep = 0; rep < 3; ++rep) {
            v4u z = {0, 0, 0, 0};
            asm volatile("s_memtime %0\n\ts_waitcnt lgkmcnt(0)" : "=s"(t0) :: "memory");
            st_sc1(ring, pr, z); drain();
            asm volatile("s_memtime %0\n\ts_waitcnt lgkmcnt(0)" : "=s"(t1) :: "memory");
            st_sc1(ring, pr + 8192, z); drain();
            asm volatile("s_memtime %0\n\ts_waitcnt lgkmcnt(0)" : "=s"(t2) :: "memory");
            best0 = min(best0, (unsigned)(t1 - t0)); best1 = min(best1, (unsigned)(t2 - t1));
        }
        if (lane == 0) ctl_st(p.ws, C_SEL, best0 <= best1 ? 1u : 2u);
    }
    bf16x16 w[40];
    {
        lds8* W = L + wave * 40960;
        const int q4 = (lane & 15) >> 2, p4 = lane & 3;
        lds8* b0 = W + (8 * g + q4) * 64 + (((p4 >> 1) ^ ((g & 1) << 1)) * 16) + (p4 & 1) * 8;
        lds8* b1 = W + (8 * g + q4) * 64 + (((2 + (p4 >> 1)) ^ ((g & 1) << 1)) * 16) + (p4 & 1) * 8;
        const size_t so = (size_t)(lane >> 2) * 8192 + (size_t)h * 256 + wave * 64 + (lane & 3) * 16;
        stage64<640>((const unsigned char*)p.wq + so, 8192, W, lane);
        lds_fence();
#pragma unroll
        for (int ks = 0; ks < 10; ++ks) { w[ks * 4 + 0] = frag16_64(b0, ks); w[ks * 4 + 1] = frag16_64(b1, ks); }
#pragma unroll
        for (int ks = 0; ks < 10; ++ks) { asm volatile("" : "+a"(w[ks * 4 + 0])); asm volatile("" : "+a"(w[ks * 4 + 1])); }
        lds_fence();
        stage64<640>((const unsigned char*)p.wk + so, 8192, W, lane);
        lds_fence();
#pragma unroll
        for (int ks = 0; ks < 10; ++ks) { w[ks * 4 + 2] = frag16_64(b0, ks); w[ks * 4 + 3] = frag16_64(b1, ks); }
#pragma unroll
        for (int ks = 0; ks < 5; ++ks) { asm volatile("" : "+a"(w[ks * 4 + 2])); asm volatile("" : "+a"(w[ks * 4 + 3])); }
        lds_fence();
    }
    if (!finish_sync(p, selbit)) return;
    __syncthreads();
    const int idx = (lane & 8) ? 0xEEEE : 0x4444;
    unsigned kxo[2];
#pragma unroll
    for (int kt = 0; kt < 2; ++kt) { const int pos = 8 * (c >> 2) + 4 * kt + (c & 3); const int hi = pos >> 4, r = pos & 15; const int tok = 4 * hi + (r & 3) + 8 * (r >> 2);
        kxo[kt] = OFF_KX + (unsigned)h * 8192 + tok * 64 + g * 16; }
    v4u kx[8];
    if (wave == 0) {
        if (!wait_ge(p.ws, C_FK + h * 128, 1u)) return;
#pragma unroll
        for (int i = 0; i < 8; ++i) kx[i] = ld_sc1(wsr, kxo[i >> 2] + (i & 3) * 2048);
    }
    const unsigned trT = L_T + (8 * g + ((lane & 15) >> 2)) * 32 + (lane & 3) * 8;
    for (int s = 0; s < NSEG; ++s) {
        if (!gather_m(p, ring, selbit, s, L, tid)) return;
        __syncthreads();
        v4u a[10];
#pragma unroll
        for (int ks = 0; ks < 10; ++ks) a[ks] = lds_rd16(L + L_M + ks * 1024 + lane * 16);
        f32x4 acc[4];
#pragma unroll
        for (int t = 0; t < 4; ++t) { acc[t] = (f32x4){0.f, 0.f, 0.f, 0.f}; asm volatile("" : "+v"(acc[t])); }
#pragma unroll
        for (int ks = 0; ks < 10; ++ks)
#pragma unroll
            for (int t = 0; t < 4; ++t) acc[t] = smf(as8(a[ks]), w[ks * 4 + t], acc[t], idx);
#pragma unroll
        for (int t = 0; t < 2; ++t) {
            const f32x4 x = fold2(acc[t], acc[2 + t]);
            v2u o = {pk2(x[0], x[1]), pk2(x[2], x[3])};
            lds_wr8(L + L_T + (wave * 32 + t * 16 + c) * 32 + g * 8, o);
        }
        __syncthreads();
        if (wave < 2) {
            bf16x8 F[4];
#pragma unroll
            for (int ks = 0; ks < 4; ++ks) F[ks] = cat8(lds_tr(L + trT + ks * 1024), lds_tr(L + trT + ks * 1024 + 128));
            if (wave == 1) {
#pragma unroll
                for (int ks = 0; ks < 4; ++ks) st_pl(wsr, c >= 8 ? OFF_KM + (unsigned)(s * NH + h) * 2048 + (c - 8) * 256 + ks * 64 + g * 16 : OOB, __builtin_bit_cast(v4u, F[ks]));
            } else if (s < NSEG - 1) {
                f32x4 dm = {0.f, 0.f, 0.f, 0.f}, d0 = dm, d1 = dm;
#pragma unroll
                for (int ks = 0; ks < 4; ++ks) { d0 = mfma16(as8(kx[ks]), F[ks], d0); d1 = mfma16(as8(kx[4 + ks]), F[ks], d1); dm = mfma16(F[ks], F[ks], dm); }
                const float NEG = -3.0e38f;
                if (g < 2) dm = (f32x4){NEG, NEG, NEG, NEG};
                float mx = fmaxf(fmaxf(fmaxf(d0[0], d0[1]), fmaxf(d0[2], d0[3])), fmaxf(fmaxf(d1[0], d1[1]), fmaxf(d1[2], d1[3])));
                mx = fmaxf(mx, fmaxf(fmaxf(dm[0], dm[1]), fmaxf(dm[2], dm[3])));
                mx = xmax_g(mx);
                const float CS = 0.08838834764831845f * 1.4426950408889634f;
                const float mc = mx * CS;
                float sum = 0.f;
#pragma unroll
                for (int i = 0; i < 4; ++i) {
                    d0[i] = __builtin_amdgcn_exp2f(d0[i] * CS - mc); d1[i] = __builtin_amdgcn_exp2f(d1[i] * CS - mc);
                    dm[i] = g < 2 ? 0.f : __builtin_amdgcn_exp2f(dm[i] * CS - mc);
                    sum += d0[i] + d1[i] + dm[i];
                }
                sum = xsum_g(sum);
                const float inv = __builtin_amdgcn_rcpf(sum);
                const unsigned pb = (unsigned)(s & 3) * RING_SLOT + RING_P + h * 640;
                v4u px = {safe(pk2(d0[0] * inv, d0[1] * inv)), safe(pk2(d0[2] * inv, d0[3] * inv)), safe(pk2(d1[0] * inv, d1[1] * inv)), safe(pk2(d1[2] * inv, d1[3] * inv))};
                st_sc1(ring, c < 8 ? physo(pb + c * 64 + g * 16, selbit) : OOB, px);
                const unsigned m0 = safe(pk2(dm[0] * inv, dm[1] * inv)), m1 = safe(pk2(dm[2] * inv, dm[3] * inv));
                const v4u pm = sw16x2(m0, m0, m1, m1);
                st_sc1(ring, (c < 8 && g == 2) ? physo(pb + 512 + c * 16, selbit) : OOB, pm);
                if (s + 1 < NSEG - 1) {
                    if (!wait_ge(p.ws, C_FK + h * 128, (unsigned)s + 2u)) return;
#pragma unroll
                    for (int i = 0; i < 8; ++i) kx[i] = ld_sc1(wsr, kxo[i >> 2] + (unsigned)(s + 1) * (NH * 8192) + (i & 3) * 2048);
                }
            }
        } else if (wave == 2) {
            v4u s4 = {SENT, SENT, SENT, SENT};
            st_sc1(ring, lane < 40 ? physo((unsigned)((s + 2) & 3) * RING_SLOT + RING_P + h * 640 + lane * 16, selbit) : OOB, s4);
        }
    }
}

DEVI void role_Y(const Params& p, lds8* L, int h, __amdgpu_buffer_rsrc_t ring, __amdgpu_buffer_rsrc_t wsr) {
    const int tid = threadIdx.x, lane = tid & 63, wave = __builtin_amdgcn_readfirstlane(tid >> 6);
    const int g = lane >> 4, c = lane & 15;
    const v4u s4 = {SENT, SENT, SENT, SENT};
    for (int i = tid; i < 1280; i += 256) st_sc1(wsr, OFF_YT + (unsigned)((i / 640) * NH + h) * 10240 + (i % 640) * 16, s4);
    unsigned selbit = 0;
    start_sync(p, tid, selbit);
    bf16x16 wv[20], wo[20];
    {
        lds8* W = L + wave * 40960;
        const int q4 = (lane & 15) >> 2, p4 = lane & 3;
        lds8* b0 = W + (8 * g + q4) * 64 + (((p4 >> 1) ^ ((g & 1) << 1)) * 16) + (p4 & 1) * 8;
        lds8* b1 = W + (8 * g + q4) * 64 + (((2 + (p4 >> 1)) ^ ((g & 1) << 1)) * 16) + (p4 & 1) * 8;
        const size_t so = (size_t)(lane >> 2) * 8192 + (size_t)h * 256 + wave * 64 + (lane & 3) * 16;
        stage64<640>((const unsigned char*)p.wv + so, 8192, W, lane);
        lds_fence();
#pragma unroll
        for (int ks = 0; ks < 10; ++ks) { wv[ks * 2 + 0] = frag16_64(b0, ks); wv[ks * 2 + 1] = frag16_64(b1, ks); }
#pragma unroll
        for (int i = 0; i < 20; ++i) asm volatile("" : "+a"(wv[i]));
        lds_fence();
        stage320((const unsigned char*)p.wo + (size_t)h * 128 * 1280 + wave * 320, W, lane);
        lds_fence();
        lds8* e0 = W + (8 * g + q4) * 320 + (((p4 >> 1) ^ ((g & 1) << 1)) * 16) + (p4 & 1) * 8;
        lds8* e1 = W + (8 * g + q4) * 320 + (((2 + (p4 >> 1)) ^ ((g & 1) << 1)) * 16) + (p4 & 1) * 8;
#pragma unroll
        for (int t = 0; t < 10; ++t)
#pragma unroll
            for (int ks = 0; ks < 2; ++ks) {
                lds8* b = ((t & 1) ? e1 : e0) + (t >> 1) * 64 + ks * 64 * 320;
                wo[t * 2 + ks] = cat16(lds_tr(b), lds_tr(b + 4 * 320), lds_tr(b + 32 * 320), lds_tr(b + 36 * 320));
            }
#pragma unroll
        for (int i = 0; i < 10; ++i) asm volatile("" : "+a"(wo[i]));
        lds_fence();
    }
    if (!finish_sync(p, selbit)) return;
    __syncthreads();
    const int idx = (lane & 8) ? 0xEEEE : 0x4444;
    const int q4 = (lane & 15) >> 2, p4 = lane & 3;
    const unsigned trV = L_T + (16 * g + 4 * (q4 >> 1) + 2 * (p4 >> 1) + (q4 & 1)) * 16 + (p4 & 1) * 8;
    for (int s = 0; s < NSEG; ++s) {
        if (!gather_m(p, ring, selbit, s, L, tid)) return;
        __syncthreads();
        v4u a[10];
#pragma unroll
        for (int ks = 0; ks < 10; ++ks) a[ks] = lds_rd16(L + L_M + ks * 1024 + lane * 16);
        f32x4 v0 = {0.f, 0.f, 0.f, 0.f}, v1 = v0;
        asm volatile("" : "+v"(v0), "+v"(v1));
#pragma unroll
        for (int ks = 0; ks < 10; ++ks) { v0 = smf(as8(a[ks]), wv[ks * 2], v0, idx); v1 = smf(as8(a[ks]), wv[ks * 2 + 1], v1, idx); }
        {
            const f32x4 x = fold2(v0, v1);
            v2u o = {pk2(x[0], x[1]), pk2(x[2], x[3])};
            lds_wr8(L + L_T + (wave * 32 + (g >> 1) * 16 + c) * 16 + (g & 1) * 8, o);
        }
        __syncthreads();
        bf16x8 av[2];
#pragma unroll
        for (int ks = 0; ks < 2; ++ks) av[ks] = cat8(lds_tr(L + trV + ks * 1024), lds_tr(L + trV + ks * 1024 + 128));
        f32x4 y[10];
#pragma unroll
        for (int t = 0; t < 10; ++t) { y[t] = (f32x4){0.f, 0.f, 0.f, 0.f}; asm volatile("" : "+v"(y[t])); }
#pragma unroll
        for (int t = 0; t < 10; ++t) { y[t] = smf(av[0], wo[t * 2], y[t], idx); y[t] = smf(av[1], wo[t * 2 + 1], y[t], idx); }
        const unsigned yb = OFF_YT + (unsigned)(s * NH + h) * 10240 + wave * 2560;
        unsigned pkd[5][2];
#pragma unroll
        for (int u = 0; u < 5; ++u) { const f32x4 x = fold2(y[2 * u], y[2 * u + 1]); pkd[u][0] = safe(pk2(x[0], x[1])); pkd[u][1] = safe(pk2(x[2], x[3])); }
#pragma unroll
        for (int k = 0; k < 2; ++k) {
            const v4u o = sw16x2(pkd[2 * k][0], pkd[2 * k + 1][0], pkd[2 * k][1], pkd[2 * k + 1][1]);
            const int tile = 4 * k + 2 * (g & 1) + (g >> 1);
            st_sc1(wsr, yb + (tile * 16 + c) * 16, o);
        }
        {
            const v4u o = sw16x2(pkd[4][0], pkd[4][0], pkd[4][1], pkd[4][1]);
            st_sc1(wsr, (g & 1) ? OOB : yb + ((8 + (g >> 1)) * 16 + c) * 16, o);
        }
        if (s + 2 < NSEG) {
            const unsigned ab = OFF_YT + (unsigned)((s + 2) * NH + h) * 10240;
            st_sc1(wsr, ab + tid * 16, s4); st_sc1(wsr, ab + 4096 + tid * 16, s4); st_sc1(wsr, tid < 128 ? ab + 8192 + tid * 16 : OOB, s4);
        }
    }
}

DEVI void role_V(const Params& p, lds8* L, int v, __amdgpu_buffer_rsrc_t ring, __amdgpu_buffer_rsrc_t wsr) {
    const int tid = threadIdx.x, lane = tid & 63, wave = __builtin_amdgcn_readfirstlane(tid >> 6);
    const int g = lane >> 4, c = lane & 15;
    const v4u s4 = {SENT, SENT, SENT, SENT};
    if (tid < 64) { const int sl = 1 + (tid >> 5), par = (tid >> 4) & 1, i = tid & 15;
        st_sc1(ring, physo(sl * RING_SLOT + RING_M + v * 256 + i * 16, (unsigned)par << 13), s4); }
    unsigned selbit = 0;
    start_sync(p, tid, selbit);
    if (!finish_sync(p, selbit)) return;
    const int col = 16 * v + 4 * ((c & 7) >> 1) + 2 * (c >> 3) + (c & 1);
    const unsigned uo = OFF_UT + (unsigned)(8 * wave) * 40960 + col * 64 + g * 16;
    const unsigned yo = OFF_YT + (unsigned)(8 * wave + g) * 10240 + col * 16;
    const unsigned po = RING_P + (8 * wave + (c >> 3)) * 640 + (c & 7) * 64 + g * 16;
    const unsigned pmo = RING_P + (8 * wave + 4 * (c >> 3) + g) * 640 + 512 + (c & 7) * 16;
    v4u u[8];
    if (!wait_ge(p.ws, C_FU + (8 * wave + (lane & 7)) * 128, 1u)) return;
#pragma unroll
    for (int i = 0; i < 8; ++i) u[i] = ld_sc1(wsr, uo + i * 40960);
    for (int s = 0; s < NSEG - 1; ++s) {
        const unsigned sb = (unsigned)(s & 3) * RING_SLOT;
        unsigned pa[5];
#pragma unroll
        for (int t = 0; t < 4; ++t) pa[t] = physo(sb + po + t * 1280, selbit);
        pa[4] = physo(sb + pmo, selbit);
        const unsigned ya = yo + (unsigned)s * (NH * 10240);
        v4u pf[5], yf[2];
        for (unsigned spins = 0;;) {
            cbar();
            yf[0] = ld_sc1(wsr, ya); yf[1] = ld_sc1(wsr, ya + 4 * 10240);
#pragma unroll
            for (int t = 0; t < 5; ++t) pf[t] = ld_sc1(ring, pa[t]);
            unsigned mx = max(umax4(yf[0]), umax4(yf[1]));
#pragma unroll
            for (int t = 0; t < 5; ++t) mx = max(mx, umax4(pf[t]));
            if (__all(mx != SENT)) break;
            if (spin_fail(spins, p.ws)) return;
        }
        f32x4 a1 = {0.f, 0.f, 0.f, 0.f}, a2 = a1;
        a1 = mfma16(as8(yf[0]), as8(pf[4]), a1); a2 = mfma16(as8(yf[1]), as8(pf[4]), a2);
#pragma unroll
        for (int t = 0; t < 4; ++t) { a1 = mfma16(as8(u[2 * t]), as8(pf[t]), a1); a2 = mfma16(as8(u[2 * t + 1]), as8(pf[t]), a2); }
        const f32x4 x = c < 8 ? a1 : a2;
        lds_wr16(L + L_RED + wave * 1024 + lane * 16, __builtin_bit_cast(v4u, x));
        __syncthreads();
        if (wave == 0) {
            f32x4 r = __builtin_bit_cast(f32x4, lds_rd16(L + L_RED + lane * 16)) + __builtin_bit_cast(f32x4, lds_rd16(L + L_RED + 1024 + lane * 16))
                    + __builtin_bit_cast(f32x4, lds_rd16(L + L_RED + 2048 + lane * 16)) + __builtin_bit_cast(f32x4, lds_rd16(L + L_RED + 3072 + lane * 16));
#pragma unroll
            for (int i = 0; i < 4; ++i) { const float t = r[i]; r[i] = t + __int_as_float(__builtin_amdgcn_update_dpp(0, __float_as_int(t), 0x128, 0xf, 0xf, false)); }
            const unsigned m0 = safe(pk2(r[0], r[1])), m1 = safe(pk2(r[2], r[3]));
            const v4u o = sw16x2(m0, m0, m1, m1);
            st_sc1(ring, (c < 8 && !(g & 1)) ? physo((unsigned)((s + 1) & 3) * RING_SLOT + RING_M + v * 256 + (g >> 1) * 128 + c * 16, selbit) : OOB, o);
        } else if (wave == 1) {
            st_sc1(ring, lane < 16 ? physo((unsigned)((s + 3) & 3) * RING_SLOT + RING_M + v * 256 + lane * 16, selbit) : OOB, s4);
        }
        if (s + 1 < NSEG - 1) {
            if (!wait_ge(p.ws, C_FU + (((s + 1) & 1) * 32 + 8 * wave + (lane & 7)) * 128, (unsigned)((s + 1) >> 1) + 1u)) return;
#pragma unroll
            for (int i = 0; i < 8; ++i) u[i] = ld_sc1(wsr, uo + (unsigned)(s + 1) * (NH * 40960) + i * 40960);
        }
    }
}

DEVI lds8* fragA32_base(lds8* W, int lane) {
    const int hi = lane >> 5, blk = (lane >> 4) & 1, q4 = (lane & 15) >> 2, p4 = lane & 3;
    return W + (8 * hi + q4) * 64 + (((2 * (p4 & 1) + blk) ^ (hi << 1)) * 16) + (p4 >> 1) * 8;
}
DEVI bf16x8 fragA32(lds8* b, int ks) { return cat8(lds_tr(b + ks * 1024), lds_tr(b + ks * 1024 + 256)); }
DEVI void dma_seg(const unsigned char* src, lds8* dst, int wave, int lane) {
#pragma unroll
    for (int i = 0; i < 10; ++i)
        __builtin_amdgcn_global_load_lds((const GAS unsigned*)(src + (wave * 10 + i) * 1024 + lane * 16), (LAS unsigned*)(dst + (wave * 10 + i) * 1024), 16, 0, 0);
}
DEVI void pack16(const f32x16& d, v4u& lo, v4u& hi) {
    lo = (v4u){pk2(d[0], d[1]), pk2(d[2], d[3]), pk2(d[4], d[5]), pk2(d[6], d[7])};
    hi = (v4u){pk2(d[8], d[9]), pk2(d[10], d[11]), pk2(d[12], d[13]), pk2(d[14], d[15])};
}

DEVI void role_XQK(const Params& p, lds8* L, int h, __amdgpu_buffer_rsrc_t wsr) {
    const int tid = threadIdx.x, lane = tid & 63, wave = __builtin_amdgcn_readfirstlane(tid >> 6);
    bf16x8 wk[40], wq[40];
    {
        lds8* W = L + wave * 40960;
        lds8* b = fragA32_base(W, lane);
        const size_t so = (size_t)(lane >> 2) * 8192 + (size_t)h * 256 + wave * 64 + (lane & 3) * 16;
        stage64<640>((const unsigned char*)p.wk + so, 8192, W, lane);
        lds_fence();
#pragma unroll
        for (int ks = 0; ks < 40; ++ks) wk[ks] = fragA32(b, ks);
#pragma unroll
        for (int ks = 0; ks < 40; ++ks) asm volatile("" : "+a"(wk[ks]));
        lds_fence();
        stage64<640>((const unsigned char*)p.wq + so, 8192, W, lane);
        lds_fence();
#pragma unroll
        for (int ks = 0; ks < 40; ++ks) wq[ks] = fragA32(b, ks);
#pragma unroll
        for (int ks = 0; ks < 20; ++ks) asm volatile("" : "+a"(wq[ks]));
        lds_fence();
    }
    __syncthreads();
    dma_seg(p.ws + OFF_HF, L, wave, lane);
    for (int s = 0; s < NSEG; ++s) {
        drain();
        __syncthreads();
        if (tid == 0 && s > 0) ctl_st(p.ws, C_FK + h * 128, (unsigned)s);
        if (s + 1 < NSEG) dma_seg(p.ws + OFF_HF + (size_t)(s + 1) * 40960, L + ((s + 1) & 1) * 40960, wave, lane);
        lds8* B = L + (s & 1) * 40960 + lane * 16;
        f32x16 dk, dq;
#pragma unroll
        for (int i = 0; i < 16; ++i) { dk[i] = 0.f; dq[i] = 0.f; }
        asm volatile("" : "+v"(dk), "+v"(dq));
#pragma unroll
        for (int ks = 0; ks < 40; ++ks) { const bf16x8 b = as8(lds_rd16(B + ks * 1024)); dk = mfma32(wk[ks], b, dk); dq = mfma32(wq[ks], b, dq); }
        const unsigned o = (unsigned)(s * NH + h) * 8192 + wave * 2048 + (lane & 31) * 64 + (lane >> 5) * 32;
        v4u lo, hi;
        pack16(dk, lo, hi); st_sc1(wsr, OFF_KX + o, lo); st_sc1(wsr, OFF_KX + o + 16, hi);
        pack16(dq, lo, hi); st_pl(wsr, OFF_QX + o, lo); st_pl(wsr, OFF_QX + o + 16, hi);
    }
    drain();
    __syncthreads();
    if (tid == 0) ctl_st(p.ws, C_FK + h * 128, (unsigned)NSEG);
}

DEVI void role_XVU(const Params& p, lds8* L, int h, int set, __amdgpu_buffer_rsrc_t wsr) {
    const int tid = threadIdx.x, lane = tid & 63, wave = __builtin_amdgcn_readfirstlane(tid >> 6);
    bf16x8 wv[40], wo[40];
    {
        lds8* W = L + wave * 40960;
        lds8* b = fragA32_base(W, lane);
        const size_t so = (size_t)(lane >> 2) * 8192 + (size_t)h * 256 + wave * 64 + (lane & 3) * 16;
        stage64<640>((const unsigned char*)p.wv + so, 8192, W, lane);
        lds_fence();
#pragma unroll
        for (int ks = 0; ks < 40; ++ks) wv[ks] = fragA32(b, ks);
#pragma unroll
        for (int ks = 0; ks < 40; ++ks) asm volatile("" : "+a"(wv[ks]));
        lds_fence();
        stage320((const unsigned char*)p.wo + (size_t)h * 128 * 1280 + wave * 320, W, lane);
        lds_fence();
        const int hi = lane >> 5, blk = (lane >> 4) & 1, q4 = (lane & 15) >> 2, p4 = lane & 3;
        lds8* e0 = W + (16 * hi + q4) * 320 + ((2 * blk + (p4 >> 1)) * 16) + (p4 & 1) * 8;
        lds8* e1 = W + (16 * hi + 8 + q4) * 320 + (((2 * blk + (p4 >> 1)) ^ 2) * 16) + (p4 & 1) * 8;
#pragma unroll
        for (int t = 0; t < 5; ++t)
#pragma unroll
            for (int kk = 0; kk < 8; ++kk) { lds8* bb = ((kk & 1) ? e1 : e0) + (kk >> 1) * 32 * 320 + t * 64; wo[t * 8 + kk] = cat8(lds_tr(bb), lds_tr(bb + 4 * 320)); }
#pragma unroll
        for (int i = 0; i < 20; ++i) asm volatile("" : "+a"(wo[i]));
        lds_fence();
    }
    __syncthreads();
    lds8* VX = L + 81920;
    dma_seg(p.ws + OFF_HF + (size_t)set * 40960, L, wave, lane);
    for (int i = 0; i < NSEG / 2; ++i) {
        const int s = 2 * i + set;
        drain();
        __syncthreads();
        if (tid == 0 && i > 0) ctl_st(p.ws, C_FU + (set * 32 + h) * 128, (unsigned)i);
        if (i + 1 < NSEG / 2) dma_seg(p.ws + OFF_HF + (size_t)(s + 2) * 40960, L + ((i + 1) & 1) * 40960, wave, lane);
        lds8* B = L + (i & 1) * 40960 + lane * 16;
        f32x16 dv;
#pragma unroll
        for (int k = 0; k < 16; ++k) dv[k] = 0.f;
        asm volatile("" : "+v"(dv));
#pragma unroll
        for (int ks = 0; ks < 40; ++ks) dv = mfma32(wv[ks], as8(lds_rd16(B + ks * 1024)), dv);
        v4u lo, hi;
        pack16(dv, lo, hi);
        lds_wr16(VX + wave * 2048 + lane * 16, lo); lds_wr16(VX + wave * 2048 + 1024 + lane * 16, hi);
        lds_fence();
        __builtin_amdgcn_s_barrier();
        cbar();
        bf16x8 av[8];
#pragma unroll
        for (int kk = 0; kk < 8; ++kk) av[kk] = as8(lds_rd16(VX + kk * 1024 + lane * 16));
        const unsigned ub = OFF_UT + (unsigned)(s * NH + h) * 40960 + (wave * 160 + (lane & 31)) * 64 + (lane >> 5) * 32;
#pragma unroll
        for (int t = 0; t < 5; ++t) {
            f32x16 d;
#pragma unroll
            for (int k = 0; k < 16; ++k) d[k] = 0.f;
            asm volatile("" : "+v"(d));
#pragma unroll
            for (int kk = 0; kk < 8; ++kk) d = mfma32(av[kk], wo[t * 8 + kk], d);
            pack16(d, lo, hi);
            st_sc1(wsr, ub + t * 2048, lo); st_sc1(wsr, ub + t * 2048 + 16, hi);
        }
    }
    drain();
    __syncthreads();
    if (tid == 0) ctl_st(p.ws, C_FU + (set * 32 + h) * 128, (unsigned)(NSEG / 2));
}

__global__ void __launch_bounds__(256, 1) k_main(Params p) {
    extern __shared__ __attribute__((aligned(16))) unsigned char smem[];
    lds8* L = (lds8*)smem;
    const int b = blockIdx.x, idx = (b >> 3) * 4 + (b & 3);
    const __amdgpu_buffer_rsrc_t wsr = mkrs(p.ws, WS_USED);
    unsigned char* rb = (unsigned char*)(((size_t)p.ws + OFF_RINGLO + (2 * MB - 1)) & ~(size_t)(2 * MB - 1));
    const __amdgpu_buffer_rsrc_t ring = mkrs(rb, 4 * 2 * RING_SLOT + 16384);
    if ((b & 4) == 0) {
        if (idx < 32) role_S(p, L, idx, ring, wsr);
        else if (idx < 64) role_Y(p, L, idx - 32, ring, wsr);
        else if (idx < 104) role_V(p, L, idx - 64, ring, wsr);
    } else {
        if (idx < 32) role_XQK(p, L, idx, wsr);
        else if (idx < 96) role_XVU(p, L, (idx - 32) & 31, (idx - 32) >> 5, wsr);
    }
}

__global__ void __launch_bounds__(256) k_att(Params p) {
    __shared__ __attribute__((aligned(16))) unsigned char smem[8 * 2048 + 4096];
    lds8* L = (lds8*)smem;
    const int tid = threadIdx.x, lane = tid & 63, wave = __builtin_amdgcn_readfirstlane(tid >> 6);
    const int g = lane >> 4, c = lane & 15;
    const int s = blockIdx.x >> 2, hg = blockIdx.x & 3;
    const __amdgpu_buffer_rsrc_t wsr = mkrs(p.ws, WS_USED);
    unsigned kxo[2];
#pragma unroll
    for (int kt = 0; kt < 2; ++kt) { const int pos = 8 * (c >> 2) + 4 * kt + (c & 3); const int hi = pos >> 4, r = pos & 15; kxo[kt] = (4 * hi + (r & 3) + 8 * (r >> 2)) * 64 + g * 16; }
#pragma unroll
    for (int hh = 0; hh < 2; ++hh) {
        const int hl = wave * 2 + hh, h = hg * 8 + hl;
        const unsigned hb = (unsigned)(s * NH + h) * 8192;
        v4u kx[8], qf[8], km[4];
#pragma unroll
        for (int i = 0; i < 8; ++i) { kx[i] = ld_pl(wsr, OFF_KX + hb + kxo[i >> 2] + (i & 3) * 2048); qf[i] = ld_pl(wsr, OFF_QX + hb + ((i >> 2) * 16 + c) * 64 + g * 16 + (i & 3) * 2048); }
#pragma unroll
        for (int ks = 0; ks < 4; ++ks) km[ks] = ld_pl(wsr, OFF_KM + (unsigned)(s * NH + h) * 2048 + (c & 7) * 256 + ks * 64 + g * 16);
#pragma unroll
        for (int qt = 0; qt < 2; ++qt) {
            f32x4 dm = {0.f, 0.f, 0.f, 0.f}, d0 = dm, d1 = dm;
#pragma unroll
            for (int ks = 0; ks < 4; ++ks) { const bf16x8 q = as8(qf[qt * 4 + ks]); d0 = mfma16(as8(kx[ks]), q, d0); d1 = mfma16(as8(kx[4 + ks]), q, d1); dm = mfma16(as8(km[ks]), q, dm); }
            const int qrow = qt * 16 + c;
            const float NEG = -3.0e38f;
            bool ok0[4], ok1[4];
#pragma unroll
            for (int i = 0; i < 4; ++i) { const int t0 = 4 * (g >> 1) + i + 16 * (g & 1); ok0[i] = t0 <= qrow; ok1[i] = t0 + 8 <= qrow;
                if (!ok0[i]) d0[i] = NEG; if (!ok1[i]) d1[i] = NEG; if (g >= 2) dm[i] = NEG; }
            float mx = fmaxf(fmaxf(fmaxf(d0[0], d0[1]), fmaxf(d0[2], d0[3])), fmaxf(fmaxf(d1[0], d1[1]), fmaxf(d1[2], d1[3])));
            mx = fmaxf(mx, fmaxf(fmaxf(dm[0], dm[1]), fmaxf(dm[2], dm[3])));
            mx = xmax_g(mx);
            const float CS = 0.08838834764831845f * 1.4426950408889634f, mc = mx * CS;
            float sum = 0.f;
#pragma unroll
            for (int i = 0; i < 4; ++i) {
                d0[i] = ok0[i] ? __builtin_amdgcn_exp2f(d0[i] * CS - mc) : 0.f; d1[i] = ok1[i] ? __builtin_amdgcn_exp2f(d1[i] * CS - mc) : 0.f;
                dm[i] = g < 2 ? __builtin_amdgcn_exp2f(dm[i] * CS - mc) : 0.f;
                sum += d0[i] + d1[i] + dm[i];
            }
            sum = xsum_g(sum);
            const float inv = __builtin_amdgcn_rcpf(sum);
            v4u px = {pk2(d0[0] * inv, d0[1] * inv), pk2(d0[2] * inv, d0[3] * inv), pk2(d1[0] * inv, d1[1] * inv), pk2(d1[2] * inv, d1[3] * inv)};
            lds_wr16(L + (hl * 2 + qt) * 1024 + lane * 16, px);
            if (g < 2) { v2u pm = {pk2(dm[0] * inv, dm[1] * inv), pk2(dm[2] * inv, dm[3] * inv)};
                lds_wr8(L + 16384 + ((hl >> 2) * 2 + qt) * 1024 + ((hl & 3) * 16 + c) * 16 + g * 8, pm); }
        }
    }
    __syncthreads();
    bf16x8 pf[20];
#pragma unroll
    for (int i = 0; i < 20; ++i) pf[i] = as8(lds_rd16(L + i * 1024 + lane * 16));
    float* part = (float*)(p.ws + OFF_PART) + (size_t)hg * 2048 * DM;
#pragma unroll 2
    for (int t = 0; t < 10; ++t) {
        const int col = wave * 160 + t * 16 + c;
        v4u z[10];
#pragma unroll
        for (int hl = 0; hl < 8; ++hl) z[hl] = ld_pl(wsr, OFF_UT + (unsigned)(s * NH + hg * 8 + hl) * 40960 + col * 64 + g * 16);
#pragma unroll
        for (int kk = 0; kk < 2; ++kk) z[8 + kk] = ld_pl(wsr, OFF_YT + (unsigned)(s * NH + hg * 8 + kk * 4 + g) * 10240 + col * 16);
        f32x4 o0 = {0.f, 0.f, 0.f, 0.f}, o1 = o0;
#pragma unroll
        for (int k = 0; k < 10; ++k) { o0 = mfma16(as8(z[k]), pf[2 * k], o0); o1 = mfma16(as8(z[k]), pf[2 * k + 1], o1); }
        *(GAS f32x4*)(part + (size_t)(s * 32 + c) * DM + wave * 160 + t * 16 + 4 * g) = o0;
        *(GAS f32x4*)(part + (size_t)(s * 32 + 16 + c) * DM + wave * 160 + t * 16 + 4 * g) = o1;
    }
}

__global__ void __launch_bounds__(256) k_ln2(Params p) {
    const int tid = threadIdx.x, row = blockIdx.x * 16 + (tid >> 4), i16 = tid & 15;
    const float* mod = (const float*)(p.ws + OFF_MOD);
    const float* part = (const float*)(p.ws + OFF_PART);
    float xv[40];
#pragma unroll
    for (int c = 0; c < 5; ++c) {
        const int col = (i16 + 16 * c) * 8;
        const v4u v = gld16(p.x + (size_t)row * DM + col);
        f32x4 a0 = {0.f, 0.f, 0.f, 0.f}, a1 = a0;
#pragma unroll
        for (int k = 0; k < 4; ++k) { a0 += *(const GAS f32x4*)(part + ((size_t)k * 2048 + row) * DM + col); a1 += *(const GAS f32x4*)(part + ((size_t)k * 2048 + row) * DM + col + 4); }
        const f32x4 g0 = *(const f32x4*)(mod + 2 * DM + col), g1 = *(const f32x4*)(mod + 2 * DM + col + 4);
#pragma unroll
        for (int j = 0; j < 4; ++j) {
            const float xl = (j & 1) ? bfhi(v[j >> 1]) : bflo(v[j >> 1]), xh = (j & 1) ? bfhi(v[2 + (j >> 1)]) : bflo(v[2 + (j >> 1)]);
            xv[c * 8 + j] = rbf16(xl + g0[j] * a0[j]); xv[c * 8 + 4 + j] = rbf16(xh + g1[j] * a1[j]);
        }
        v4u o = {pk2(xv[c * 8], xv[c * 8 + 1]), pk2(xv[c * 8 + 2], xv[c * 8 + 3]), pk2(xv[c * 8 + 4], xv[c * 8 + 5]), pk2(xv[c * 8 + 6], xv[c * 8 + 7])};
        *(GAS v4u*)(p.ws + OFF_X1 + ((size_t)row * DM + col) * 2) = o;
    }
    ln_mod_store(xv, mod + 3 * DM, mod + 4 * DM, p.ws + OFF_H2F, row, i16);
}

__global__ void __launch_bounds__(256, 1) k_ffn1(Params p) {
    extern __shared__ __attribute__((aligned(16))) unsigned char smem[];
    lds8* L = (lds8*)smem;
    const int tid = threadIdx.x, lane = tid & 63, wave = __builtin_amdgcn_readfirstlane(tid >> 6);
    const int cb = blockIdx.x % 27, rb = blockIdx.x / 27;
    bf16x8 w[40];
    {
        lds8* W = L + wave * 40960;
        const int pc = lane & 3;
        const unsigned char* src = (const unsigned char*)(pc < 2 ? p.w1 : p.w3) + (size_t)(lane >> 2) * (FF * 2) + (cb * 64 + wave * 16) * 2 + (pc & 1) * 16;
        stage64<640>(src, FF * 2, W, lane);
        lds_fence();
        const int hi = lane >> 5, blk = (lane >> 4) & 1, q4 = (lane & 15) >> 2, p4 = lane & 3;
        lds8* b = W + (8 * hi + q4) * 64 + (((2 * blk + (p4 & 1)) ^ (hi << 1)) * 16) + (p4 >> 1) * 8;
#pragma unroll
        for (int ks = 0; ks < 40; ++ks) w[ks] = fragA32(b, ks);
        lds_fence();
    }
    __syncthreads();
    dma_seg(p.ws + OFF_H2F + (size_t)(rb * 8) * 40960, L, wave, lane);
    for (int i = 0; i < 8; ++i) {
        const int s = rb * 8 + i;
        drain();
        __syncthreads();
        if (i + 1 < 8) dma_seg(p.ws + OFF_H2F + (size_t)(s + 1) * 40960, L + ((i + 1) & 1) * 40960, wave, lane);
        lds8* B = L + (i & 1) * 40960 + lane * 16;
        f32x16 d;
#pragma unroll
        for (int k = 0; k < 16; ++k) d[k] = 0.f;
#pragma unroll
        for (int ks = 0; ks < 40; ++ks) d = mfma32(w[ks], as8(lds_rd16(B + ks * 1024)), d);
        float o[8];
#pragma unroll
        for (int k = 0; k < 8; ++k) { const float gt = rbf16(d[k]), up = rbf16(d[8 + k]); o[k] = rbf16(gt * __builtin_amdgcn_rcpf(1.f + __expf(-gt))) * up; }
        v4u ov = {pk2(o[0], o[1]), pk2(o[2], o[3]), pk2(o[4], o[5]), pk2(o[6], o[7])};
        *(GAS v4u*)(p.ws + OFF_GF + ((size_t)s * 108 + cb * 4 + wave) * 1024 + lane * 16) = ov;
    }
}

__global__ void __launch_bounds__(256, 1) k_ffn2(Params p) {
    extern __shared__ __attribute__((aligned(16))) unsigned char smem[];
    lds8* L = (lds8*)smem;
    const int tid = threadIdx.x, lane = tid & 63, wave = __builtin_amdgcn_readfirstlane(tid >> 6);
    const int cb = blockIdx.x % 10, rb = blockIdx.x / 10;
    bf16x8 w[54];
    {
        lds8* W = L + wave * 28672;
        lds8* b = fragA32_base(W, lane);
#pragma unroll
        for (int t = 0; t < 2; ++t) {
            const unsigned char* src = (const unsigned char*)p.w2 + (size_t)(wave * 432 + (lane >> 2)) * (DM * 2) + (cb * 64 + t * 32) * 2 + (lane & 3) * 16;
            stage64<432>(src, DM * 2, W, lane);
            lds_fence();
#pragma unroll
            for (int ks = 0; ks < 27; ++ks) w[t * 27 + ks] = fragA32(b, ks);
            lds_fence();
        }
    }
    __syncthreads();
    const float* mod = (const float*)(p.ws + OFF_MOD);
    const int s_lo = rb * 3, s_hi = min(NSEG, s_lo + 3);
    for (int s = s_lo; s < s_hi; ++s) {
        const unsigned char* gsrc = p.ws + OFF_GF + ((size_t)s * 108 + wave * 27) * 1024 + lane * 16;
        v4u gfr[27];
#pragma unroll
        for (int ks = 0; ks < 27; ++ks) gfr[ks] = gld16(gsrc + ks * 1024);
        f32x16 d0, d1;
#pragma unroll
        for (int k = 0; k < 16; ++k) { d0[k] = 0.f; d1[k] = 0.f; }
#pragma unroll
        for (int ks = 0; ks < 27; ++ks) { d0 = mfma32(w[ks], as8(gfr[ks]), d0); d1 = mfma32(w[27 + ks], as8(gfr[ks]), d1); }
        __syncthreads();
#pragma unroll
        for (int k = 0; k < 4; ++k) {
            f32x4 a = {d0[4 * k], d0[4 * k + 1], d0[4 * k + 2], d0[4 * k + 3]}, bq = {d1[4 * k], d1[4 * k + 1], d1[4 * k + 2], d1[4 * k + 3]};
            lds_wr16(L + (wave * 8 + k) * 1024 + lane * 16, __builtin_bit_cast(v4u, a));
            lds_wr16(L + (wave * 8 + 4 + k) * 1024 + lane * 16, __builtin_bit_cast(v4u, bq));
        }
        __syncthreads();
        f32x4 r0 = {0.f, 0.f, 0.f, 0.f}, r1 = r0;
#pragma unroll
        for (int k = 0; k < 4; ++k) { r0 += __builtin_bit_cast(f32x4, lds_rd16(L + (k * 8 + wave * 2) * 1024 + lane * 16)); r1 += __builtin_bit_cast(f32x4, lds_rd16(L + (k * 8 + wave * 2 + 1) * 1024 + lane * 16)); }
        const int row = s * 32 + (lane & 31), col = cb * 64 + (wave >> 1) * 32 + (lane >> 5) * 16 + (wave & 1) * 8;
        const v4u xv = gld16(p.ws + OFF_X1 + ((size_t)row * DM + col) * 2);
        const f32x4 g0 = *(const f32x4*)(mod + 5 * DM + col), g1 = *(const f32x4*)(mod + 5 * DM + col + 4);
        v4u o;
        o.x = pk2(bflo(xv.x) + g0[0] * r0[0], bfhi(xv.x) + g0[1] * r0[1]); o.y = pk2(bflo(xv.y) + g0[2] * r0[2], bfhi(xv.y) + g0[3] * r0[3]);
        o.z = pk2(bflo(xv.z) + g1[0] * r1[0], bfhi(xv.z) + g1[1] * r1[1]); o.w = pk2(bflo(xv.w) + g1[2] * r1[2], bfhi(xv.w) + g1[3] * r1[3]);
        *(GAS v4u*)(p.out + (size_t)row * DM + col) = o;
    }
}

extern "C" void kernel_launch(void* const* d_in, const int* in_sizes, int n_in, void* d_out, int out_size, void* d_ws, size_t ws_size, hipStream_t stream) {
    Params p;
    p.x = (const u16*)d_in[0]; p.mem0 = (const u16*)d_in[1]; p.wq = (const u16*)d_in[2]; p.wk = (const u16*)d_in[3]; p.wv = (const u16*)d_in[4]; p.wo = (const u16*)d_in[5];
    p.w1 = (const u16*)d_in[8]; p.w3 = (const u16*)d_in[9]; p.w2 = (const u16*)d_in[10]; p.c = (const u16*)d_in[11]; p.wmod = (const u16*)d_in[12];
    p.ws = (unsigned char*)d_ws; p.out = (u16*)d_out;
    (void)hipFuncSetAttribute((const void*)k_main, hipFuncAttributeMaxDynamicSharedMemorySize, LDS_MAIN);
    (void)hipFuncSetAttribute((const void*)k_ffn1, hipFuncAttributeMaxDynamicSharedMemorySize, LDS_MAIN);
    (void)hipFuncSetAttribute((const void*)k_ffn2, hipFuncAttributeMaxDynamicSharedMemorySize, 4 * 28672);
    (void)hipMemsetAsync(d_ws, 0, CTL_BYTES, stream);
    k_mod<<<60, 256, 0, stream>>>(p);
    k_ln1<<<128, 256, 0, stream>>>(p);
    k_main<<<256, 256, LDS_MAIN, stream>>>(p);
    k_att<<<256, 256, 0, stream>>>(p);
    k_ln2<<<128, 256, 0, stream>>>(p);
    k_ffn1<<<216, 256, LDS_MAIN, stream>>>(p);
    k_ffn2<<<220, 256, 4 * 28672, stream>>>(p);
}
```

```cpp
#include <hip/hip_runtime.h>
#include <hip/hip_bf16.h>

#define LAS __attribute__((address_space(3)))
#define GAS __attribute__((address_space(1)))
#define DEVI __device__ __forceinline__
typedef unsigned short u16;
typedef __bf16 bf16x4 __attribute__((ext_vector_type(4)));
typedef __bf16 bf16x8 __attribute__((ext_vector_type(8)));
typedef __bf16 bf16x16 __attribute__((ext_vector_type(16)));
typedef float f32x4 __attribute__((ext_vector_type(4)));
typedef float f32x16 __attribute__((ext_vector_type(16)));
typedef unsigned v2u __attribute__((ext_vector_type(2)));
typedef unsigned v4u __attribute__((ext_vector_type(4)));
typedef unsigned v8u __attribute__((ext_vector_type(8)));
typedef __bf16 bf16x2 __attribute__((ext_vector_type(2)));
typedef LAS unsigned char lds8;
typedef __amdgpu_buffer_rsrc_t rsrc_t;
#define RLX_AGENT __ATOMIC_RELAXED, __HIP_MEMORY_SCOPE_AGENT

constexpr int DM = 640, NH = 32, NSEG = 64, FF = 1728;
constexpr unsigned MB = 1u << 20;
constexpr unsigned C_READY = 0, C_ABORT = 128, C_SEL = 256, C_DBG = 768;
constexpr unsigned C_FK = 1024, C_FU = C_FK + 32 * 128, C_FS = C_FU + 32 * 128, C_PZ = C_FS + 64 * 128, C_H = C_PZ + 64 * 128, C_F1 = C_H + 64 * 128;
constexpr unsigned OFF_MOD = C_F1 + 64 * 128;
constexpr unsigned CTL_BYTES = OFF_MOD + 3840 * 4;
constexpr unsigned OFF_HF = 5 * MB, OFF_KX = 8 * MB, OFF_QX = 24 * MB, OFF_PX = 40 * MB, OFF_UT = 64 * MB;
constexpr unsigned OFF_X1F = 164 * MB, OFF_ST = 170 * MB, OFF_GF = 176 * MB;
constexpr unsigned OFF_NEARLO = 192 * MB, WS_USED = 240 * MB;
constexpr unsigned N_HS = 163840, N_HV = 163840 + 8192, N_PROBE = 163840 + 16384, N_YT = 1 * MB;
#define C1_TICKS 12
#define C2_TICKS 12
#define PEN_TICKS 60
static_assert(CTL_BYTES <= OFF_HF && CTL_BYTES % 16 == 0, "ctl map");
constexpr unsigned RING_SLOT = 40960, RING_M = 0, RING_P = 16384;
constexpr unsigned SENT = 0xFFFFFFFFu;
constexpr unsigned OOB = 0xFFFFFF00u;
constexpr int NSYNC = 104;
constexpr unsigned SPIN_CAP = 400000u;
constexpr int LDS_MAIN = 160 * 1024;

struct Params {
    const u16 *x, *mem0, *wq, *wk, *wv, *wo, *w1, *w3, *w2, *c, *wmod;
    unsigned char* ws;
    u16* out;
};

DEVI float bf2f(unsigned h) { return __uint_as_float(h << 16); }
DEVI float bflo(unsigned u) { return __uint_as_float(u << 16); }
DEVI float bfhi(unsigned u) { return __uint_as_float(u & 0xffff0000u); }
DEVI unsigned pk2(float a, float b) { bf16x2 r; r[0] = (__bf16)a; r[1] = (__bf16)b; return __builtin_bit_cast(unsigned, r); }
DEVI float rbf16(float a) { return (float)(__bf16)a; }
DEVI unsigned safe(unsigned u) { return u < 0xFFFFFFFEu ? u : 0xFFFFFFFEu; }
DEVI rsrc_t mkrs(const void* p, unsigned bytes) { return __builtin_amdgcn_make_buffer_rsrc((void*)p, 0, bytes, 0x00020000); }
DEVI v4u ld_sc1(rsrc_t r, unsigned off) { return __builtin_amdgcn_raw_buffer_load_b128(r, off, 0, 16); }
DEVI void st_sc1(rsrc_t r, unsigned off, v4u v) { __builtin_amdgcn_raw_buffer_store_b128(v, r, off, 0, 16); }
DEVI v4u ld_pl(rsrc_t r, unsigned off) { return __builtin_amdgcn_raw_buffer_load_b128(r, off, 0, 0); }
DEVI void st_pl(rsrc_t r, unsigned off, v4u v) { __builtin_amdgcn_raw_buffer_store_b128(v, r, off, 0, 0); }
DEVI v4u gld16(const void* p) { return *(const GAS v4u*)p; }
DEVI v4u lds_rd16(lds8* p) { return *(LAS v4u*)p; }
DEVI void lds_wr16(lds8* p, v4u v) { *(LAS v4u*)p = v; }
DEVI void lds_wr8(lds8* p, v2u v) { *(LAS v2u*)p = v; }
DEVI v2u lds_tr(lds8* p) { return __builtin_bit_cast(v2u, __builtin_amdgcn_ds_read_tr16_b64_v4bf16((LAS bf16x4*)p)); }
DEVI bf16x8 as8(v4u v) { return __builtin_bit_cast(bf16x8, v); }
DEVI bf16x8 cat8(v2u a, v2u b) { v4u v = {a.x, a.y, b.x, b.y}; return __builtin_bit_cast(bf16x8, v); }
DEVI bf16x16 cat16(v2u a, v2u b, v2u c, v2u d) { v8u v = {a.x, a.y, b.x, b.y, c.x, c.y, d.x, d.y}; return __builtin_bit_cast(bf16x16, v); }
DEVI f32x4 mfma16(bf16x8 a, bf16x8 b, f32x4 c) { return __builtin_amdgcn_mfma_f32_16x16x32_bf16(a, b, c, 0, 0, 0); }
DEVI f32x16 mfma32(bf16x8 a, bf16x8 b, f32x16 c) { return __builtin_amdgcn_mfma_f32_32x32x16_bf16(a, b, c, 0, 0, 0); }
DEVI f32x4 smf(bf16x8 a, bf16x16 b, f32x4 c, int idx) { return __builtin_amdgcn_smfmac_f32_16x16x64_bf16(a, b, c, idx, 0, 0); }
DEVI unsigned umax3(unsigned a, unsigned b, unsigned c) { return max(max(a, b), c); }
DEVI unsigned umax4(v4u v) { return max(max(v.x, v.y), max(v.z, v.w)); }
DEVI void cbar() { asm volatile("" ::: "memory"); }
DEVI void drain() { asm volatile("s_waitcnt vmcnt(0)" ::: "memory"); }
DEVI void sw32(unsigned a, unsigned b, unsigned& o0, unsigned& o1) { auto r = __builtin_amdgcn_permlane32_swap(a, b, false, false); o0 = r[0]; o1 = r[1]; }
DEVI void sw16(unsigned a, unsigned b, unsigned& o0, unsigned& o1) { auto r = __builtin_amdgcn_permlane16_swap(a, b, false, false); o0 = r[0]; o1 = r[1]; }
DEVI v4u sw16x2(unsigned a0, unsigned b0, unsigned a1, unsigned b1) { unsigned x, z, y, w; sw16(a0, b0, x, z); sw16(a1, b1, y, w); return (v4u){x, y, z, w}; }
DEVI float xmax_g(float v) {
    unsigned a, b; sw32(__float_as_uint(v), __float_as_uint(v), a, b); v = fmaxf(__uint_as_float(a), __uint_as_float(b));
    sw16(__float_as_uint(v), __float_as_uint(v), a, b); return fmaxf(__uint_as_float(a), __uint_as_float(b));
}
DEVI float xsum_g(float v) {
    unsigned a, b; sw32(__float_as_uint(v), __float_as_uint(v), a, b); v = __uint_as_float(a) + __uint_as_float(b);
    sw16(__float_as_uint(v), __float_as_uint(v), a, b); return __uint_as_float(a) + __uint_as_float(b);
}
DEVI f32x4 fold2(f32x4 a, f32x4 b) {
    f32x4 r;
#pragma unroll
    for (int i = 0; i < 4; ++i) { unsigned x, y; sw32(__float_as_uint(a[i]), __float_as_uint(b[i]), x, y); r[i] = __uint_as_float(x) + __uint_as_float(y); }
    return r;
}
DEVI unsigned physo(unsigned lo, unsigned nb) { return (((lo >> 13) << 14) | (((lo >> 7) & 0x2000u) ^ ((nb & 1u) << 13)) | (lo & 8191u)) + (nb & ~1u); }
DEVI unsigned near_off(const unsigned char* ws) { return (unsigned)((((size_t)ws + OFF_NEARLO + (4 * MB - 1)) & ~(size_t)(4 * MB - 1)) - (size_t)ws); }
DEVI unsigned rtime() { return (unsigned)__builtin_amdgcn_s_memrealtime(); }
DEVI unsigned ld4_sc1(rsrc_t r, unsigned off) { return __builtin_amdgcn_raw_buffer_load_b32(r, off, 0, 16); }
DEVI void st4_sc1(rsrc_t r, unsigned off, unsigned v) { __builtin_amdgcn_raw_buffer_store_b32(v, r, off, 0, 16); }
DEVI int wave_max_i(int v) {
#pragma unroll
    for (int o = 32; o >= 1; o >>= 1) v = max(v, __shfl_xor(v, o));
    return __builtin_amdgcn_readfirstlane(v);
}
DEVI float vmax3(float a, float b, float c) { float r; asm("v_max3_f32 %0, %1, %2, %3" : "=v"(r) : "v"(a), "v"(b), "v"(c)); return r; }
DEVI float vmax2(float a, float b) { float r; asm("v_max_f32 %0, %1, %2" : "=v"(r) : "v"(a), "v"(b)); return r; }
DEVI unsigned ctl_ld(unsigned char* ws, unsigned off) { return __hip_atomic_load((GAS unsigned*)(ws + off), RLX_AGENT); }
DEVI void ctl_st(unsigned char* ws, unsigned off, unsigned v) { __hip_atomic_store((GAS unsigned*)(ws + off), v, RLX_AGENT); }
DEVI void spin_tick(unsigned& spins, unsigned& dead, unsigned char* ws) {
    ++spins;
    if ((spins & 1023u) == 0u) { if (spins > SPIN_CAP) { ctl_st(ws, C_ABORT, 1u); dead = 1u; } else dead = __builtin_amdgcn_readfirstlane(ctl_ld(ws, C_ABORT)); }
}
DEVI unsigned wait_ge(unsigned char* ws, unsigned off, unsigned want, unsigned& dead) {
    unsigned v = ctl_ld(ws, off);
    for (unsigned spins = 0; !__all(v >= want) && !dead;) {
        __builtin_amdgcn_s_sleep(2);
        spin_tick(spins, dead, ws);
        v = ctl_ld(ws, off);
    }
    return v;
}

template <int NR>
DEVI void stage64(const unsigned char* src  , size_t ld, lds8* dst, int lane) {
    constexpr int NI = NR / 16;
    v4u v[NI];
#pragma unroll
    for (int it = 0; it < NI; ++it) v[it] = gld16(src + (size_t)it * 16 * ld);
    const int r = lane >> 2, pc = lane & 3;
    lds8* d = dst + r * 64 + ((pc ^ (((r >> 3) & 1) << 1)) * 16);
#pragma unroll
    for (int it = 0; it < NI; ++it) lds_wr16(d + it * 1024, v[it]);
}
DEVI void stage320(const unsigned char* src, lds8* dst, int lane) {
    v4u v[40];
#pragma unroll
    for (int it = 0; it < 40; ++it) { const int ci = it * 64 + lane, r = ci / 20, ch = ci % 20; v[it] = gld16(src + (size_t)r * 1280 + ch * 16); }
#pragma unroll
    for (int it = 0; it < 40; ++it) { const int ci = it * 64 + lane, r = ci / 20, ch = ci % 20; lds_wr16(dst + r * 320 + ((ch ^ (((r >> 3) & 1) << 1)) * 16), v[it]); }
}
DEVI void lds_fence() { asm volatile("s_waitcnt lgkmcnt(0)" ::: "memory"); }

#define LSTAMP(s, k, v) do {} while (0)
#define CSTAMP(s, k) do {} while (0)
#define STAMP_DUMP() do {} while (0)
#define FAILCNT(k) do {} while (0)
#define FAILDUMP(k) do {} while (0)

#define SY_DELAY 250
constexpr unsigned NPZ = 20;
constexpr int L_M = 0;
constexpr int L_T = 10240;
constexpr int L_W = 14336;
constexpr int L_RED = 0;
DEVI unsigned lds_rd4(lds8* p) { return *(volatile LAS unsigned*)p; }
DEVI void lds_wr4(lds8* p, unsigned v) { *(volatile LAS unsigned*)p = v; }
DEVI void bar() { asm volatile("s_waitcnt lgkmcnt(0)\n\ts_barrier" ::: "memory"); }
DEVI f32x4 asf(v4u v) { return __builtin_bit_cast(f32x4, v); }
DEVI v4u asu(f32x4 v) { return __builtin_bit_cast(v4u, v); }
#define MFMA_V(d, a, b) asm volatile("v_mfma_f32_16x16x32_bf16 %0, %1, %2, %0" : "+v"(d) : "v"(a), "v"(b))
#define MFMA_V0(d, a, b) asm volatile("v_mfma_f32_16x16x32_bf16 %0, %1, %2, 0" : "=&v"(d) : "v"(a), "v"(b))
#define MFMA_VC(d, a, b, c) asm volatile("v_mfma_f32_16x16x32_bf16 %0, %1, %2, %3" : "=&v"(d) : "v"(a), "v"(b), "v"(c))

DEVI void publish_add(const Params& p, unsigned off) {
    drain();
    __syncthreads();
    if (threadIdx.x == 0) __hip_atomic_fetch_add((GAS unsigned*)(p.ws + off), 1u, RLX_AGENT);
}
DEVI unsigned get_nb(const Params& p, unsigned& dead) {
    return near_off(p.ws) | ((__builtin_amdgcn_readfirstlane(wait_ge(p.ws, C_SEL, 1u, dead)) - 1u) & 1u);
}
DEVI int wave_max_fast(int v) {
    v = max(v, __builtin_amdgcn_update_dpp(v, v, 0x128, 0xf, 0xf, false));
    v = max(v, __builtin_amdgcn_update_dpp(v, v, 0x124, 0xf, 0xf, false));
    v = max(v, __builtin_amdgcn_update_dpp(v, v, 0x122, 0xf, 0xf, false));
    v = max(v, __builtin_amdgcn_update_dpp(v, v, 0x121, 0xf, 0xf, false));
    return max(max(__builtin_amdgcn_readlane(v, 0), __builtin_amdgcn_readlane(v, 16)), max(__builtin_amdgcn_readlane(v, 32), __builtin_amdgcn_readlane(v, 48)));
}
DEVI void wait_prog(const Params& p, unsigned off, unsigned need, unsigned& seen, unsigned& dead) {
    if (seen < need) seen = (unsigned)(-wave_max_fast(-(int)wait_ge(p.ws, off, need, dead)));
}
DEVI unsigned hint_max(const Params& p, rsrc_t wr, unsigned ho, int s, unsigned now, unsigned& dead) {
    ho += (unsigned)(s & 1) * 4u;
    unsigned hv = ld4_sc1(wr, ho);
    for (unsigned spins = 0; !__all((hv >> 24) >= (unsigned)s + 1u) && !dead;) {
        spin_tick(spins, dead, p.ws);
        cbar();
        hv = ld4_sc1(wr, ho);
    }
    const int rel = (hv >> 24) == (unsigned)s + 1u ? ((int)((hv - now) << 8) >> 8) : -100000;
    return now + (unsigned)min(wave_max_fast(rel), 1000);
}
DEVI void wait_until(unsigned t) { while ((int)(rtime() - t) < 0) {} }
DEVI void plan_next(rsrc_t wr, unsigned hint_off, int s, unsigned x, int dv, unsigned t_pub, bool clean, unsigned& target, unsigned& dur, lds8* L) {
    LSTAMP(s, 0, target); LSTAMP(s, 1, t_pub); LSTAMP(s, 2, x); LSTAMP(s, 3, (unsigned)dv); LSTAMP(s, 4, dur);
    if (clean) dur = min(t_pub - target, dur + 8u);
    if ((int)(t_pub - x) > 6) x = t_pub + PEN_TICKS;
    target = x + (unsigned)(C1_TICKS + C2_TICKS + dv);
    const unsigned now = rtime();
    if ((int)(target - now) < 0) target = now;
    st4_sc1(wr, hint_off + (unsigned)((s + 1) & 1) * 4u, ((unsigned)(s + 2) << 24) | ((target + dur) & 0xFFFFFFu));
}

DEVI bf16x16 frag16_64(lds8* b) { return cat16(lds_tr(b), lds_tr(b + 128), lds_tr(b + 2048), lds_tr(b + 2048 + 128)); }
DEVI lds8* frag16_base(lds8* W, int lane, int t) {
    const int g = lane >> 4, q4 = (lane & 15) >> 2, p4 = lane & 3;
    const int row = 16 * (g >> 1) + 4 * (g & 1) + (q4 & 1) + 8 * (q4 >> 1);
    return W + row * 64 + (((2 * t + (p4 >> 1)) ^ ((q4 >> 1) << 1)) * 16) + (p4 & 1) * 8;
}
DEVI int slot_ks(int j, int wave) { return j == 0 ? wave : j == 1 ? 4 + wave : j < 5 ? ((wave + j - 1) & 3) : j < 8 ? 4 + ((wave + j - 4) & 3) : j; }

DEVI void gather_m(const Params& p, rsrc_t wr, unsigned o0, unsigned o2, lds8* Lm, lds8* Lm2, lds8* Lf, v4u& a0, v4u& a1, unsigned& dead, unsigned& nfail) {
    a0 = ld_sc1(wr, o0); a1 = ld_sc1(wr, o0 + 4096);
    v4u c = ld_sc1(wr, o2);
    lds_wr16(Lm, a0); lds_wr16(Lm + 4096, a1); lds_wr16(Lm2, c);
    if (__builtin_expect(!__all(umax3(umax4(a0), umax4(a1), umax4(c)) != SENT), 0)) {
        unsigned spins = 0;
        do {
            spin_tick(spins, dead, p.ws);
            cbar();
            a0 = ld_sc1(wr, o0); a1 = ld_sc1(wr, o0 + 4096); c = ld_sc1(wr, o2);
        } while (!__all(umax3(umax4(a0), umax4(a1), umax4(c)) != SENT) && !dead);
        lds_wr16(Lm, a0); lds_wr16(Lm + 4096, a1); lds_wr16(Lm2, c);
        lds_wr4(Lf, 1u);
        FAILCNT(0);
    }
}

DEVI void softmax_store(rsrc_t wr, f32x4 d0, f32x4 d1, f32x4 dm, unsigned pxo, unsigned pmo) {
    float mx = vmax3(vmax3(d0[0], d0[1], d0[2]), vmax3(d0[3], d1[0], d1[1]), vmax3(d1[2], d1[3], dm[0]));
    mx = vmax3(mx, vmax2(dm[1], dm[2]), dm[3]);
    { unsigned x, y; sw32(__float_as_uint(mx), __float_as_uint(mx), x, y); mx = vmax2(__uint_as_float(x), __uint_as_float(y));
      sw16(__float_as_uint(mx), __float_as_uint(mx), x, y); mx = vmax2(__uint_as_float(x), __uint_as_float(y)); }
    const float CS = 0.08838834764831845f * 1.4426950408889634f;
    const float mc = mx * CS;
    d0 = d0 * CS - mc; d1 = d1 * CS - mc; dm = dm * CS - mc;
#pragma unroll
    for (int i = 0; i < 4; ++i) { d0[i] = __builtin_amdgcn_exp2f(d0[i]); d1[i] = __builtin_amdgcn_exp2f(d1[i]); dm[i] = __builtin_amdgcn_exp2f(dm[i]); }
    const f32x4 sv = d0 + d1 + dm;
    const float sum = xsum_g((sv[0] + sv[1]) + (sv[2] + sv[3]));
    const float inv = __builtin_amdgcn_rcpf(sum);
    d0 *= inv; d1 *= inv; dm *= inv;
    const v4u px = {safe(pk2(d0[0], d0[1])), safe(pk2(d0[2], d0[3])), safe(pk2(d1[0], d1[1])), safe(pk2(d1[2], d1[3]))};
    st_sc1(wr, pxo, px);
    const unsigned m0 = safe(pk2(dm[0], dm[1])), m1 = safe(pk2(dm[2], dm[3]));
    st_sc1(wr, pmo, sw16x2(m0, m0, m1, m1));
}

DEVI void role_S(const Params& p, lds8* L, int h, rsrc_t wr) {
    const int tid = threadIdx.x, lane = tid & 63, wave = __builtin_amdgcn_readfirstlane(tid >> 6);
    const int g = lane >> 4, c = lane & 15;
    const v4u s4 = {SENT, SENT, SENT, SENT};
    unsigned dead = 0, nfail = 0;
    {
        const unsigned n0 = near_off(p.ws);
        if (tid < 160) { const int sl = tid / 80, par = (tid / 40) & 1, i = tid % 40; st_sc1(wr, physo(sl * RING_SLOT + RING_P + h * 640 + i * 16, n0 | par), s4); }
        if (tid >= 192 && tid < 196) st4_sc1(wr, physo(N_HS + h * 128 + (tid & 2) * 2, n0 | (tid & 1)), 0u);
        publish_add(p, C_READY);
        wait_until(rtime() + SY_DELAY);
    }
    bf16x16 w[40];
    {
        lds8* W = L + wave * 40960;
        lds8* b0 = frag16_base(W, lane, 0);
        lds8* b1 = frag16_base(W, lane, 1);
        const size_t so = (size_t)(lane >> 2) * 8192 + (size_t)h * 256 + wave * 64 + (lane & 3) * 16;
        stage64<640>((const unsigned char*)p.wq + so, 8192, W, lane);
        lds_fence();
#pragma unroll
        for (int j = 0; j < 10; ++j) { const int ko = slot_ks(j, wave) * 4096; w[j * 4 + 0] = frag16_64(b0 + ko); w[j * 4 + 1] = frag16_64(b1 + ko); }
#pragma unroll
        for (int j = 0; j < 10; ++j) { asm volatile("" : "+a"(w[j * 4 + 0])); asm volatile("" : "+a"(w[j * 4 + 1])); }
        lds_fence();
        stage64<640>((const unsigned char*)p.wk + so, 8192, W, lane);
        lds_fence();
#pragma unroll
        for (int j = 0; j < 10; ++j) { const int ko = slot_ks(j, wave) * 4096; w[j * 4 + 2] = frag16_64(b0 + ko); w[j * 4 + 3] = frag16_64(b1 + ko); }
#pragma unroll
        for (int j = 0; j < 6; ++j) { asm volatile("" : "+a"(w[j * 4 + 2])); asm volatile("" : "+a"(w[j * 4 + 3])); }
        lds_fence();
    }
    wait_ge(p.ws, C_READY, NSYNC, dead);
    const unsigned nb = get_nb(p, dead);
    __syncthreads();
    lds_wr4(L + L_W, 0u); lds_wr4(L + L_W + 4, 0u);
    const int idx = (lane & 8) ? 0xEEEE : 0x4444;
    const unsigned kxo = OFF_KX + (unsigned)h * 8192 + lane * 16;
    const unsigned qxo = OFF_QX + (unsigned)h * 8192 + (wave == 2 ? 1024 : 0) + lane * 16;
    v4u kx[8], qf[4];
#pragma unroll
    for (int i = 0; i < 8; ++i) kx[i] = (v4u){0u, 0u, 0u, 0u};
#pragma unroll
    for (int i = 0; i < 4; ++i) qf[i] = (v4u){0u, 0u, 0u, 0u};
    unsigned fk_seen = 0, dur = 150u, target = 0;
    lds8* const trT = L + L_T + (8 * g + ((lane & 15) >> 2)) * 32 + (lane & 3) * 8;
    lds8* const Lm = L + L_M + tid * 16;
    lds8* const Lm2 = L + L_M + 8192 + (tid & 127) * 16 + (tid >> 7) * 8192;
    lds8* const Tw = L + L_T + (wave * 32 + c) * 32 + g * 8;
    lds8* ra[3];
#pragma unroll
    for (int i = 0; i < 3; ++i) ra[i] = L + L_M + ((wave + 1 + i) & 3) * 1024 + lane * 16;
    const unsigned hint_off = tid == 192 ? physo(N_HS + h * 128, nb) : OOB;
    const unsigned fs_off = tid == 128 ? C_FS + h * 128 : OOB;
    const unsigned ho = physo(N_HS + lane * 128, nb), vo = lane < 40 ? physo(N_HV + lane * 128, nb) : OOB;
    const float NEG = -1.0e30f;
    const float ng = g < 2 ? NEG : 0.f;
    const f32x4 negv = {ng, ng, ng, ng};
    f32x4 mk0, mk1;
#pragma unroll
    for (int i = 0; i < 4; ++i) { const int t0 = 4 * (g >> 1) + i + 16 * (g & 1), qrow = (wave == 2 ? 16 : 0) + c; mk0[i] = t0 <= qrow ? 0.f : NEG; mk1[i] = t0 + 8 <= qrow ? 0.f : NEG; }
    for (int s = 0; s < NSEG; ++s) {
        const unsigned sb = (unsigned)(s & 3) * RING_SLOT;
        unsigned pxo = OOB, pmo = OOB;
        if (wave < 3) {
            wait_prog(p, C_FK + h * 128, (unsigned)s + 1u, fk_seen, dead);
#pragma unroll
            for (int i = 0; i < 8; ++i) kx[i] = ld_sc1(wr, kxo + (unsigned)s * (NH * 8192) + (i & 3) * 2048 + (i >> 2) * 1024);
            if (wave == 0) {
                if (s < NSEG - 1) {
                    pxo = c < 8 ? physo(sb + RING_P + h * 640 + g * 128 + c * 16, nb) : OOB;
                    pmo = (c < 8 && g == 2) ? physo(sb + RING_P + h * 640 + 512 + c * 16, nb) : OOB;
                }
            } else {
#pragma unroll
                for (int i = 0; i < 4; ++i) qf[i] = ld_sc1(wr, qxo + (unsigned)s * (NH * 8192) + i * 2048);
                pxo = OFF_PX + (unsigned)(s * NH + h) * 2560 + (wave - 1) * 1024 + lane * 16;
                pmo = g == 2 ? OFF_PX + (unsigned)(s * NH + h) * 2560 + 2048 + (wave - 1) * 256 + c * 16 : OOB;
            }
#pragma unroll
            for (int i = 0; i < 8; ++i) asm volatile("" : "+v"(kx[i]));
#pragma unroll
            for (int i = 0; i < 4; ++i) asm volatile("" : "+v"(qf[i]));
        }
        unsigned o0 = physo(sb + RING_M + tid * 16, nb), o2 = tid < 128 ? o0 + 16384 : OOB;
        asm volatile("" : "+v"(pxo), "+v"(pmo), "+v"(o0), "+v"(o2));
        f32x4 acc[4];
#pragma unroll
        for (int t = 0; t < 4; ++t) { acc[t] = (f32x4){0.f, 0.f, 0.f, 0.f}; asm volatile("" : "+v"(acc[t])); }
        if (wave == 3 && s > 0) wait_until(target);
        bar();
        v4u a[10];
        if (s == 0) { target = rtime(); st4_sc1(wr, hint_off, (1u << 24) | ((target + dur) & 0xFFFFFFu)); }
        gather_m(p, wr, o0, o2, Lm, Lm2, L + L_W + 4, a[0], a[1], dead, nfail);
#pragma unroll
        for (int t = 0; t < 4; ++t) acc[t] = smf(as8(a[0]), w[t], acc[t], idx);
        __builtin_amdgcn_sched_barrier(0);
        bar();
#pragma unroll
        for (int j = 2; j < 5; ++j) a[j] = lds_rd16(ra[j - 2]);
#pragma unroll
        for (int j = 5; j < 8; ++j) a[j] = lds_rd16(ra[j - 5] + 4096);
        a[8] = lds_rd16(L + L_M + 8192 + lane * 16); a[9] = lds_rd16(L + L_M + 9216 + lane * 16);
        st4_sc1(wr, fs_off, (unsigned)s);
        __builtin_amdgcn_sched_barrier(0);
#pragma unroll
        for (int j = 1; j < 10; ++j)
#pragma unroll
            for (int t = 0; t < 4; ++t) { const int tt = (t >> 1) + 2 * (t & 1); acc[tt] = smf(as8(a[j]), w[j * 4 + tt], acc[tt], idx); }
#pragma unroll
        for (int t = 0; t < 2; ++t) {
            const f32x4 x = fold2(acc[t], acc[2 + t]);
            const v2u o = {pk2(x[0], x[1]), pk2(x[2], x[3])};
            lds_wr8(Tw + t * 512, o);
        }
        bar();
        if (wave == 0) {
            bf16x8 F[4];
#pragma unroll
            for (int ks = 0; ks < 4; ++ks) F[ks] = cat8(lds_tr(trT + ks * 1024), lds_tr(trT + ks * 1024 + 128));
            f32x4 dm, d0, d1;
            MFMA_V0(d0, kx[0], F[0]); MFMA_V0(d1, kx[4], F[0]); MFMA_VC(dm, F[0], F[0], negv);
#pragma unroll
            for (int ks = 1; ks < 4; ++ks) { MFMA_V(d0, kx[ks], F[ks]); MFMA_V(d1, kx[4 + ks], F[ks]); MFMA_V(dm, F[ks], F[ks]); }
            asm volatile("s_nop 7" : "+v"(dm), "+v"(d0), "+v"(d1));
            softmax_store(wr, d0, d1, dm, pxo, pmo);
            cbar();
            lds_wr4(L + L_W, rtime() | 1u);
        } else if (wave < 3) {
            bf16x8 F[4];
#pragma unroll
            for (int ks = 0; ks < 4; ++ks) F[ks] = cat8(lds_tr(trT + ks * 1024), lds_tr(trT + ks * 1024 + 128));
            f32x4 dm, d0, d1;
            MFMA_VC(d0, kx[0], qf[0], mk0); MFMA_VC(d1, kx[4], qf[0], mk1); MFMA_VC(dm, F[0], qf[0], negv);
#pragma unroll
            for (int ks = 1; ks < 4; ++ks) { MFMA_V(d0, kx[ks], qf[ks]); MFMA_V(d1, kx[4 + ks], qf[ks]); MFMA_V(dm, F[ks], qf[ks]); }
            asm volatile("s_nop 7" : "+v"(dm), "+v"(d0), "+v"(d1));
            softmax_store(wr, d0, d1, dm, pxo, pmo);
        } else if (s < NSEG - 1) {
            const int dvl = (int)ld4_sc1(wr, vo);
            const unsigned x = hint_max(p, wr, ho, s, target, dead);
            const int dv = wave_max_fast(dvl);
            st_sc1(wr, lane < 40 ? physo((unsigned)((s + 2) & 3) * RING_SLOT + RING_P + h * 640 + lane * 16, nb) : OOB, s4);
            unsigned tp;
            do { tp = __builtin_amdgcn_readfirstlane(lds_rd4(L + L_W)); } while (tp == 0u);
            const bool clean = __builtin_amdgcn_readfirstlane(lds_rd4(L + L_W + 4)) == 0u;
            lds_wr4(L + L_W, 0u); lds_wr4(L + L_W + 4, 0u);
            plan_next(wr, hint_off, s, x, dv, tp, clean, target, dur, L);
        }
    }
    drain();
    __syncthreads();
    st4_sc1(wr, fs_off, (unsigned)NSEG);
    FAILDUMP(0);
    STAMP_DUMP();
}

DEVI void role_Y(const Params& p, lds8* L, int h, rsrc_t wr) {
    const int tid = threadIdx.x, lane = tid & 63, wave = __builtin_amdgcn_readfirstlane(tid >> 6);
    const int g = lane >> 4, c = lane & 15;
    const v4u s4 = {SENT, SENT, SENT, SENT};
    unsigned dead = 0, nfail = 0;
    {
        const unsigned n0 = near_off(p.ws);
        for (int i = tid; i < 2560; i += 256) { const int k = i / 640; st_sc1(wr, physo(N_YT + (unsigned)((k >> 1) * NH + h) * 10240 + (i % 640) * 16, n0 | (k & 1)), s4); }
        if (tid < 4) st4_sc1(wr, physo(N_HS + (32 + h) * 128 + (tid & 2) * 2, n0 | (tid & 1)), 0u);
        publish_add(p, C_READY);
        wait_until(rtime() + SY_DELAY);
    }
    bf16x16 wv[20], wo[20];
    const int q4 = (lane & 15) >> 2, p4 = lane & 3;
    {
        lds8* W = L + wave * 40960;
        lds8* b0 = frag16_base(W, lane, 0);
        lds8* b1 = frag16_base(W, lane, 1);
        const size_t so = (size_t)(lane >> 2) * 8192 + (size_t)h * 256 + wave * 64 + (lane & 3) * 16;
        stage64<640>((const unsigned char*)p.wv + so, 8192, W, lane);
        lds_fence();
#pragma unroll
        for (int j = 0; j < 10; ++j) { const int ko = slot_ks(j, wave) * 4096; wv[j * 2 + 0] = frag16_64(b0 + ko); wv[j * 2 + 1] = frag16_64(b1 + ko); }
#pragma unroll
        for (int i = 0; i < 20; ++i) asm volatile("" : "+a"(wv[i]));
        lds_fence();
        stage320((const unsigned char*)p.wo + (size_t)h * 128 * 1280 + wave * 320, W, lane);
        lds_fence();
        lds8* e0 = W + (8 * g + q4) * 320 + (((p4 >> 1) ^ ((g & 1) << 1)) * 16) + (p4 & 1) * 8;
        lds8* e1 = W + (8 * g + q4) * 320 + (((2 + (p4 >> 1)) ^ ((g & 1) << 1)) * 16) + (p4 & 1) * 8;
#pragma unroll
        for (int t = 0; t < 10; ++t)
#pragma unroll
            for (int ks = 0; ks < 2; ++ks) {
                lds8* b = ((t & 1) ? e1 : e0) + (t >> 1) * 64 + ks * 64 * 320;
                wo[t * 2 + ks] = cat16(lds_tr(b), lds_tr(b + 4 * 320), lds_tr(b + 32 * 320), lds_tr(b + 36 * 320));
            }
#pragma unroll
        for (int i = 0; i < 12; ++i) asm volatile("" : "+a"(wo[i]));
        lds_fence();
    }
    wait_ge(p.ws, C_READY, NSYNC, dead);
    const unsigned nb = get_nb(p, dead);
    __syncthreads();
    const int idx = (lane & 8) ? 0xEEEE : 0x4444;
    lds8* const trV = L + L_T + (16 * g + 4 * (q4 >> 1) + 2 * (p4 >> 1) + (q4 & 1)) * 16 + (p4 & 1) * 8;
    lds8* const Lm = L + L_M + tid * 16;
    lds8* const Lm2 = L + L_M + 8192 + (tid & 127) * 16 + (tid >> 7) * 8192;
    lds8* const Tw = L + L_T + (wave * 32 + (g >> 1) * 16 + c) * 16 + (g & 1) * 8;
    lds8* ra[3];
#pragma unroll
    for (int i = 0; i < 3; ++i) ra[i] = L + L_M + ((wave + 1 + i) & 3) * 1024 + lane * 16;
    const unsigned hint_off = tid == 192 ? physo(N_HS + (32 + h) * 128, nb) : OOB;
    const unsigned fs_off = tid == 128 ? C_FS + (32 + h) * 128 : OOB;
    const unsigned ho = physo(N_HS + lane * 128, nb), vo = lane < 40 ? physo(N_HV + lane * 128, nb) : OOB;
    unsigned dur = 150u, target = 0;
    lds_wr4(L + L_W + 4, 0u);
    for (int s = 0; s < NSEG; ++s) {
        const unsigned yb = N_YT + (unsigned)(s * NH + h) * 10240 + wave * 2560;
        unsigned yo[3];
#pragma unroll
        for (int k = 0; k < 2; ++k) yo[k] = physo(yb + ((4 * k + 2 * (g & 1) + (g >> 1)) * 16 + c) * 16, nb);
        yo[2] = (g & 1) ? OOB : physo(yb + ((8 + (g >> 1)) * 16 + c) * 16, nb);
        unsigned o0 = physo((unsigned)(s & 3) * RING_SLOT + RING_M + tid * 16, nb), o2 = tid < 128 ? o0 + 16384 : OOB;
        asm volatile("" : "+v"(yo[0]), "+v"(yo[1]), "+v"(yo[2]), "+v"(o0), "+v"(o2));
        f32x4 v0 = {0.f, 0.f, 0.f, 0.f}, v1 = v0;
        asm volatile("" : "+v"(v0), "+v"(v1));
        f32x4 y[10];
#pragma unroll
        for (int t = 0; t < 10; ++t) { y[t] = (f32x4){0.f, 0.f, 0.f, 0.f}; asm volatile("" : "+v"(y[t])); }
        if (wave == 3 && s > 0) wait_until(target);
        bar();
        v4u a[10];
        if (s == 0) { target = rtime(); st4_sc1(wr, hint_off, (1u << 24) | ((target + dur) & 0xFFFFFFu)); }
        gather_m(p, wr, o0, o2, Lm, Lm2, L + L_W + 4, a[0], a[1], dead, nfail);
        bar();
#pragma unroll
        for (int j = 2; j < 5; ++j) a[j] = lds_rd16(ra[j - 2]);
#pragma unroll
        for (int j = 5; j < 8; ++j) a[j] = lds_rd16(ra[j - 5] + 4096);
        a[8] = lds_rd16(L + L_M + 8192 + lane * 16); a[9] = lds_rd16(L + L_M + 9216 + lane * 16);
        st4_sc1(wr, fs_off, (unsigned)s);
        __builtin_amdgcn_sched_barrier(0);
#pragma unroll
        for (int j = 0; j < 10; ++j) { v0 = smf(as8(a[j]), wv[j * 2], v0, idx); v1 = smf(as8(a[j]), wv[j * 2 + 1], v1, idx); }
        {
            const f32x4 x = fold2(v0, v1);
            const v2u o = {pk2(x[0], x[1]), pk2(x[2], x[3])};
            lds_wr8(Tw, o);
        }
        bar();
        bf16x8 av[2];
#pragma unroll
        for (int ks = 0; ks < 2; ++ks) av[ks] = cat8(lds_tr(trV + ks * 1024), lds_tr(trV + ks * 1024 + 128));
#pragma unroll
        for (int t = 0; t < 10; ++t) { y[t] = smf(av[0], wo[t * 2], y[t], idx); y[t] = smf(av[1], wo[t * 2 + 1], y[t], idx); }
        unsigned pkd[5][2];
#pragma unroll
        for (int u = 0; u < 5; ++u) { const f32x4 x = fold2(y[2 * u], y[2 * u + 1]); pkd[u][0] = safe(pk2(x[0], x[1])); pkd[u][1] = safe(pk2(x[2], x[3])); }
#pragma unroll
        for (int k = 0; k < 2; ++k) st_sc1(wr, yo[k], sw16x2(pkd[2 * k][0], pkd[2 * k + 1][0], pkd[2 * k][1], pkd[2 * k + 1][1]));
        st_sc1(wr, yo[2], sw16x2(pkd[4][0], pkd[4][0], pkd[4][1], pkd[4][1]));
        cbar();
        if (wave == 3) {
            const unsigned tp = rtime();
            const bool clean = __builtin_amdgcn_readfirstlane(lds_rd4(L + L_W + 4)) == 0u;
            lds_wr4(L + L_W + 4, 0u);
            if (s < NSEG - 1) {
                const int dvl = (int)ld4_sc1(wr, vo);
                const unsigned x = hint_max(p, wr, ho, s, target, dead);
                plan_next(wr, hint_off, s, x, wave_max_fast(dvl), tp, clean, target, dur, L);
            }
        } else if (s + 2 < NSEG) {
            const unsigned ab = N_YT + (unsigned)((s + 2) * NH + h) * 10240;
#pragma unroll
            for (int k = 0; k < 4; ++k) { const int i = k * 192 + tid; st_sc1(wr, i < 640 ? physo(ab + i * 16, nb) : OOB, s4); }
        }
    }
    drain();
    __syncthreads();
    st4_sc1(wr, fs_off, (unsigned)NSEG);
    FAILDUMP(0);
    STAMP_DUMP();
}

DEVI void role_V(const Params& p, lds8* L, int v, rsrc_t wr) {
    const int tid = threadIdx.x, lane = tid & 63, wave = __builtin_amdgcn_readfirstlane(tid >> 6);
    const int g = lane >> 4, c = lane & 15;
    const v4u s4 = {SENT, SENT, SENT, SENT};
    unsigned dead = 0, nfail = 0;
    {
        const unsigned n0 = near_off(p.ws);
        if (tid < 64) { const int sl = 1 + (tid >> 5), par = (tid >> 4) & 1, i = tid & 15; st_sc1(wr, physo(sl * RING_SLOT + RING_M + v * 256 + i * 16, n0 | par), s4); }
        else if (tid < 96) {
            const int par = (tid >> 4) & 1, i = tid & 15;
            const v4u x = gld16(p.mem0 + (i & 7) * DM + 16 * v + 8 * (i >> 3));
            const v4u o = {safe(x.x), safe(x.y), safe(x.z), safe(x.w)};
            st_sc1(wr, physo(RING_M + v * 256 + i * 16, n0 | par), o);
        } else if (tid < 98) st4_sc1(wr, physo(N_HV + v * 128, n0 | (tid & 1)), 100u);
        publish_add(p, C_READY);
        if (v == 0 && wave == 0) {
            unsigned long long t0, t1, t2; unsigned best0 = ~0u, best1 = ~0u;
            const unsigned pr = physo(N_PROBE, n0) + (lane & 7) * 16;
            for (int rep = 0; rep < 8; ++rep) {
                const v4u z = {0, 0, 0, 0};
                asm volatile("s_memtime %0\n\ts_waitcnt lgkmcnt(0)" : "=s"(t0) :: "memory");
                st_sc1(wr, pr, z); drain();
                asm volatile("s_memtime %0\n\ts_waitcnt lgkmcnt(0)" : "=s"(t1) :: "memory");
                st_sc1(wr, pr + 8192, z); drain();
                asm volatile("s_memtime %0\n\ts_waitcnt lgkmcnt(0)" : "=s"(t2) :: "memory");
                best0 = min(best0, (unsigned)(t1 - t0)); best1 = min(best1, (unsigned)(t2 - t1));
            }
            if (lane == 0) ctl_st(p.ws, C_SEL, best0 <= best1 ? 1u : 2u);
        }
    }
    wait_ge(p.ws, C_READY, NSYNC, dead);
    const unsigned nb = get_nb(p, dead);
    const unsigned uo = OFF_UT + (unsigned)(8 * wave) * 40960 + v * 1024 + lane * 16;
    const unsigned yo = N_YT + (unsigned)(8 * wave + g) * 10240 + v * 256 + c * 16;
    const unsigned po = RING_P + (8 * wave + (c >> 3)) * 640 + g * 128 + (c & 7) * 16;
    const unsigned pmo = RING_P + (8 * wave + 4 * (c >> 3) + g) * 640 + 512 + (c & 7) * 16;
    const unsigned ho = physo(N_HS + lane * 128, nb);
    unsigned dprev = 100u, fu = 0u;
    for (int s = 0; s < NSEG - 1; ++s) {
        v4u u[8];
        wait_prog(p, C_FU + (8 * wave + (lane & 7)) * 128, (unsigned)s + 1u, fu, dead);
#pragma unroll
        for (int i = 0; i < 8; ++i) u[i] = ld_sc1(wr, uo + (unsigned)s * (NH * 40960) + i * 40960);
        const unsigned sb = (unsigned)(s & 3) * RING_SLOT;
        unsigned pa[7];
#pragma unroll
        for (int t = 0; t < 4; ++t) pa[t] = physo(sb + po + t * 1280, nb);
        pa[4] = physo(sb + pmo, nb);
        pa[5] = physo(yo + (unsigned)s * (NH * 10240), nb); pa[6] = physo(yo + (unsigned)s * (NH * 10240) + 4 * 10240, nb);
        unsigned mo = (c < 8 && !(g & 1)) ? physo((unsigned)((s + 1) & 3) * RING_SLOT + RING_M + v * 256 + (g >> 1) * 128 + c * 16, nb) : OOB;
#pragma unroll
        for (int t = 0; t < 7; ++t) asm volatile("" : "+v"(pa[t]));
        asm volatile("" : "+v"(mo));
#pragma unroll
        for (int i = 0; i < 8; ++i) asm volatile("" : "+v"(u[i]));
        unsigned t_r = 0;
        if (wave == 0) { const unsigned now = rtime(); t_r = hint_max(p, wr, ho, s, now, dead) + C1_TICKS; const unsigned n2 = rtime(); if ((int)(t_r - n2) < 0) t_r = n2; wait_until(t_r); }
        bar();
        v4u pf[7];
        f32x4 a1, a2;
        for (unsigned spins = 0;;) {
            cbar();
            pf[5] = ld_sc1(wr, pa[5]); pf[6] = ld_sc1(wr, pa[6]); pf[4] = ld_sc1(wr, pa[4]);
#pragma unroll
            for (int t = 0; t < 4; ++t) pf[t] = ld_sc1(wr, pa[t]);
            __builtin_amdgcn_sched_barrier(0);
            MFMA_V0(a1, pf[5], pf[4]); MFMA_V0(a2, pf[6], pf[4]);
            unsigned mx = umax3(umax4(pf[5]), umax4(pf[6]), umax4(pf[4]));
            __builtin_amdgcn_sched_barrier(0);
#pragma unroll
            for (int t = 0; t < 4; ++t) { MFMA_V(a1, u[2 * t], pf[t]); MFMA_V(a2, u[2 * t + 1], pf[t]); mx = max(mx, umax4(pf[t])); __builtin_amdgcn_sched_barrier(0); }
            if (__builtin_expect(__all(mx != SENT) || dead, 1)) break;
            if (spins == 0) FAILCNT(1);
            spin_tick(spins, dead, p.ws);
        }
        asm volatile("s_nop 7" : "+v"(a1), "+v"(a2));
        const f32x4 x = c < 8 ? a1 : a2;
        lds_wr16(L + L_RED + wave * 1024 + lane * 16, asu(x));
        bar();
        if (wave == 0) {
            f32x4 r = (x + asf(lds_rd16(L + L_RED + 1024 + lane * 16))) + (asf(lds_rd16(L + L_RED + 2048 + lane * 16)) + asf(lds_rd16(L + L_RED + 3072 + lane * 16)));
#pragma unroll
            for (int i = 0; i < 4; ++i) { const float t = r[i]; r[i] = t + __int_as_float(__builtin_amdgcn_update_dpp(0, __float_as_int(t), 0x128, 0xf, 0xf, false)); }
            const unsigned m0 = safe(pk2(r[0], r[1])), m1 = safe(pk2(r[2], r[3]));
            st_sc1(wr, mo, sw16x2(m0, m0, m1, m1));
            cbar();
            const unsigned tp = rtime();
            dprev = min(tp - t_r, dprev + 4u);
            LSTAMP(s, 0, t_r); LSTAMP(s, 1, tp); LSTAMP(s, 2, dprev);
            st4_sc1(wr, lane == 0 ? physo(N_HV + v * 128, nb) : OOB, dprev);
        } else if (wave == 1) {
            st_sc1(wr, lane < 16 ? physo((unsigned)((s + 3) & 3) * RING_SLOT + RING_M + v * 256 + lane * 16, nb) : OOB, s4);
        }
    }
    FAILDUMP(1);
    STAMP_DUMP();
}

DEVI lds8* fragA32_base(lds8* W, int lane) {
    const int hi = lane >> 5, blk = (lane >> 4) & 1, q4 = (lane & 15) >> 2, p4 = lane & 3;
    return W + (8 * hi + q4) * 64 + (((2 * (p4 & 1) + blk) ^ (hi << 1)) * 16) + (p4 >> 1) * 8;
}
DEVI bf16x8 fragA32(lds8* b, int ks) { return cat8(lds_tr(b + ks * 1024), lds_tr(b + ks * 1024 + 256)); }
DEVI void dma_piece(const unsigned char* src_lane, lds8* dst, int piece) {
    __builtin_amdgcn_global_load_lds((const GAS unsigned*)(src_lane + piece * 1024), (LAS unsigned*)(dst + piece * 1024), 16, 0, 16);
}
DEVI void pack16(const f32x16& d, v4u& lo, v4u& hi) {
    lo = (v4u){pk2(d[0], d[1]), pk2(d[2], d[3]), pk2(d[4], d[5]), pk2(d[6], d[7])};
    hi = (v4u){pk2(d[8], d[9]), pk2(d[10], d[11]), pk2(d[12], d[13]), pk2(d[14], d[15])};
}

DEVI void role_XQK(const Params& p, lds8* L, int h, rsrc_t wr) {
    const int tid = threadIdx.x, lane = tid & 63, wave = __builtin_amdgcn_readfirstlane(tid >> 6);
    unsigned dead = 0;
    bf16x8 wk[40], wq[40];
    {
        lds8* W = L + wave * 40960;
        lds8* b = fragA32_base(W, lane);
        const size_t so = (size_t)(lane >> 2) * 8192 + (size_t)h * 256 + wave * 64 + (lane & 3) * 16;
        stage64<640>((const unsigned char*)p.wk + so, 8192, W, lane);
        lds_fence();
#pragma unroll
        for (int ks = 0; ks < 40; ++ks) wk[ks] = fragA32(b, ks);
#pragma unroll
        for (int ks = 0; ks < 40; ++ks) asm volatile("" : "+a"(wk[ks]));
        lds_fence();
        stage64<640>((const unsigned char*)p.wq + so, 8192, W, lane);
        lds_fence();
#pragma unroll
        for (int ks = 0; ks < 40; ++ks) wq[ks] = fragA32(b, ks);
#pragma unroll
        for (int ks = 0; ks < 12; ++ks) asm volatile("" : "+a"(wq[ks]));
        lds_fence();
    }
    wait_ge(p.ws, C_H + (lane & 1) * 128, 1u, dead);
    __syncthreads();
    const unsigned char* hsrc = p.ws + OFF_HF + wave * 10240 + lane * 16;
#pragma unroll
    for (int i = 0; i < 10; ++i) dma_piece(hsrc, L + wave * 10240, i);
    const unsigned fo = tid == 0 ? C_FK + h * 128 : OOB;
    const int tk = lane & 31;
    const unsigned kso = ((tk >> 3) & 1) * 1024 + (lane >> 5) * 512 + ((((tk >> 2) & 1) * 2 + (tk >> 4)) * 4 + (tk & 3)) * 16;
    const unsigned qso = (tk >> 4) * 1024 + (lane >> 5) * 512 + (tk & 15) * 16;
    for (int s = 0; s < NSEG; ++s) {
        if (s == 1) wait_ge(p.ws, C_H + lane * 128, 1u, dead);
        if (s <= 1) drain(); else asm volatile("s_waitcnt vmcnt(5)" ::: "memory");
        bar();
        if (s == 1) st4_sc1(wr, fo, 1u);
        LSTAMP(s, 0, rtime());
        CSTAMP(s, 2);
        const unsigned char* nsrc = hsrc + (size_t)min(s + 1, NSEG - 1) * 40960;
        lds8* ndst = L + ((s + 1) & 1) * 40960 + wave * 10240;
        lds8* B = L + (s & 1) * 40960 + lane * 16;
        f32x16 dk, dq;
#pragma unroll
        for (int i = 0; i < 16; ++i) { dk[i] = 0.f; dq[i] = 0.f; }
        v4u bf[2][4];
#pragma unroll
        for (int i = 0; i < 4; ++i) bf[0][i] = lds_rd16(B + i * 1024);
#pragma unroll
        for (int ch = 0; ch < 10; ++ch) {
#pragma unroll
            for (int i = 0; i < 4; ++i) {
                dk = mfma32(wk[ch * 4 + i], as8(bf[ch & 1][i]), dk); dq = mfma32(wq[ch * 4 + i], as8(bf[ch & 1][i]), dq);
                __builtin_amdgcn_sched_barrier(0);
                if (i == 0 && ch + 1 < 10) {
#pragma unroll
                    for (int k = 0; k < 4; ++k) bf[(ch + 1) & 1][k] = lds_rd16(B + ((ch + 1) * 4 + k) * 1024);
                }
                if (ch < 5 && (i & 1)) dma_piece(nsrc, ndst, ch * 2 + (i >> 1));
                __builtin_amdgcn_sched_barrier(0);
            }
            if (ch == 5) {
                CSTAMP(s, 3);
                asm volatile("s_waitcnt vmcnt(10)\n\ts_barrier" ::: "memory");
                st4_sc1(wr, fo, (unsigned)s);
                CSTAMP(s, 4);
                __builtin_amdgcn_sched_barrier(0);
            }
        }
        CSTAMP(s, 5);
        const unsigned o = (unsigned)(s * NH + h) * 8192 + wave * 2048;
        v4u lo, hi;
        pack16(dk, lo, hi); st_sc1(wr, OFF_KX + o + kso, lo); st_sc1(wr, OFF_KX + o + kso + 256, hi);
        pack16(dq, lo, hi); st_sc1(wr, OFF_QX + o + qso, lo); st_sc1(wr, OFF_QX + o + qso + 256, hi);
        CSTAMP(s, 6);
    }
    drain();
    __syncthreads();
    st4_sc1(wr, fo, (unsigned)NSEG);
    STAMP_DUMP();
}

DEVI void role_XVU(const Params& p, lds8* L, int h, rsrc_t wr) {
    const int tid = threadIdx.x, lane = tid & 63, wave = __builtin_amdgcn_readfirstlane(tid >> 6);
    unsigned dead = 0;
    bf16x8 wv[40], wo[40];
    {
        lds8* W = L + wave * 40960;
        lds8* b = fragA32_base(W, lane);
        const size_t so = (size_t)(lane >> 2) * 8192 + (size_t)h * 256 + wave * 64 + (lane & 3) * 16;
        stage64<640>((const unsigned char*)p.wv + so, 8192, W, lane);
        lds_fence();
#pragma unroll
        for (int ks = 0; ks < 40; ++ks) wv[ks] = fragA32(b, ks);
#pragma unroll
        for (int ks = 0; ks < 40; ++ks) asm volatile("" : "+a"(wv[ks]));
        lds_fence();
        stage320((const unsigned char*)p.wo + (size_t)h * 128 * 1280 + wave * 320, W, lane);
        lds_fence();
        const int hi = lane >> 5, blk = (lane >> 4) & 1, q4 = (lane & 15) >> 2, p4 = lane & 3;
        lds8* e0 = W + (16 * hi + q4) * 320 + ((2 * blk + (p4 >> 1)) * 16) + (p4 & 1) * 8;
        lds8* e1 = W + (16 * hi + 8 + q4) * 320 + (((2 * blk + (p4 >> 1)) ^ 2) * 16) + (p4 & 1) * 8;
#pragma unroll
        for (int t = 0; t < 5; ++t)
#pragma unroll
            for (int kk = 0; kk < 8; ++kk) { lds8* bb = ((kk & 1) ? e1 : e0) + (kk >> 1) * 32 * 320 + t * 64; wo[t * 8 + kk] = cat8(lds_tr(bb), lds_tr(bb + 4 * 320)); }
#pragma unroll
        for (int i = 0; i < 12; ++i) asm volatile("" : "+a"(wo[i]));
        lds_fence();
    }
    wait_ge(p.ws, C_H + (lane & 1) * 128, 1u, dead);
    __syncthreads();
    lds8* VX = L + 81920;
    const unsigned char* hsrc = p.ws + OFF_HF + wave * 10240 + lane * 16;
#pragma unroll
    for (int i = 0; i < 10; ++i) dma_piece(hsrc, L + wave * 10240, i);
    const unsigned fo = tid == 0 ? C_FU + h * 128 : OOB;
    for (int s = 0; s < NSEG; ++s) {
        if (s == 1) wait_ge(p.ws, C_H + lane * 128, 1u, dead);
        if (s <= 1) drain(); else asm volatile("s_waitcnt vmcnt(11)" ::: "memory");
        bar();
        if (s == 1) st4_sc1(wr, fo, 1u);
        LSTAMP(s, 0, rtime());
        const unsigned char* nsrc = hsrc + (size_t)min(s + 1, NSEG - 1) * 40960;
        lds8* ndst = L + ((s + 1) & 1) * 40960 + wave * 10240;
        lds8* B = L + (s & 1) * 40960 + lane * 16;
        f32x16 dv;
#pragma unroll
        for (int k = 0; k < 16; ++k) dv[k] = 0.f;
        v4u bf[2][5];
#pragma unroll
        for (int k = 0; k < 5; ++k) bf[0][k] = lds_rd16(B + k * 1024);
#pragma unroll
        for (int ch = 0; ch < 8; ++ch) {
#pragma unroll
            for (int k = 0; k < 5; ++k) {
                dv = mfma32(wv[ch * 5 + k], as8(bf[ch & 1][k]), dv);
                __builtin_amdgcn_sched_barrier(0);
                if (k == 0 && ch + 1 < 8) {
#pragma unroll
                    for (int k2 = 0; k2 < 5; ++k2) bf[(ch + 1) & 1][k2] = lds_rd16(B + ((ch + 1) * 5 + k2) * 1024);
                }
                if (ch < 5 && (k == 2 || k == 4)) dma_piece(nsrc, ndst, ch * 2 + (k >> 2));
                __builtin_amdgcn_sched_barrier(0);
            }
        }
        v4u lo, hi;
        pack16(dv, lo, hi);
        lds_wr16(VX + wave * 2048 + lane * 16, lo); lds_wr16(VX + wave * 2048 + 1024 + lane * 16, hi);
        asm volatile("s_waitcnt vmcnt(10)" ::: "memory");
        bar();
        st4_sc1(wr, fo, (unsigned)s);
        bf16x8 av[8];
#pragma unroll
        for (int kk = 0; kk < 8; ++kk) av[kk] = as8(lds_rd16(VX + kk * 1024 + lane * 16));
        const unsigned ub = OFF_UT + (unsigned)(s * NH + h) * 40960 + (wave * 10 + ((lane >> 4) & 1)) * 1024 + (lane >> 5) * 512 + (lane & 15) * 16;
        f32x16 d[2];
#pragma unroll
        for (int t = 0; t < 6; ++t) {
            if (t < 5) {
#pragma unroll
                for (int k = 0; k < 16; ++k) d[t & 1][k] = 0.f;
#pragma unroll
                for (int kk = 0; kk < 8; ++kk) d[t & 1] = mfma32(av[kk], wo[t * 8 + kk], d[t & 1]);
            }
            if (t > 0) {
                pack16(d[(t - 1) & 1], lo, hi);
                st_sc1(wr, ub + (t - 1) * 2048, lo); st_sc1(wr, ub + (t - 1) * 2048 + 256, hi);
            }
            if (t > 0 && t < 5) {
#pragma unroll
                for (int k = 0; k < 8; ++k) { __builtin_amdgcn_sched_group_barrier(0x8, 1, 0); __builtin_amdgcn_sched_group_barrier(0x2, 4, 0); }
            }
            __builtin_amdgcn_sched_barrier(0);
        }
    }
    drain();
    __syncthreads();
    st4_sc1(wr, fo, (unsigned)NSEG);
    STAMP_DUMP();
}

DEVI void mod_block(const Params& p, lds8* L, rsrc_t wr, int blk) {
    LAS float* sc = (LAS float*)L;
    LAS float* red = (LAS float*)(L + 4096);
    const int tid = threadIdx.x;
    const int cg = tid & 7, ksl = tid >> 3, n0 = blk * 64 + cg * 8;
    v4u v[20];
#pragma unroll
    for (int i = 0; i < 20; ++i) v[i] = gld16(p.wmod + (size_t)(ksl * 20 + i) * 3840 + n0);
    __syncthreads();
    for (int i = tid; i < DM; i += 256) { const float x = bf2f(p.c[i]); sc[i] = rbf16(x / (1.f + __expf(-x))); }
    __syncthreads();
    float acc[8] = {0, 0, 0, 0, 0, 0, 0, 0};
#pragma unroll
    for (int i = 0; i < 20; ++i) {
        const float s = sc[ksl * 20 + i];
#pragma unroll
        for (int j = 0; j < 4; ++j) { acc[2 * j] += s * bflo(v[i][j]); acc[2 * j + 1] += s * bfhi(v[i][j]); }
    }
#pragma unroll
    for (int j = 0; j < 8; ++j) red[ksl * 65 + cg * 8 + j] = acc[j];
    __syncthreads();
    if (tid < 64) { float t = 0; for (int k = 0; k < 32; ++k) t += red[k * 65 + tid]; st4_sc1(wr, OFF_MOD + (blk * 64 + tid) * 4, __float_as_uint(rbf16(t)) | 1u); }
}
template <int N>
DEVI void mod_load(const Params& p, rsrc_t wr, const unsigned (&eo)[N], f32x4 (&out)[N], unsigned& dead) {
    for (unsigned spins = 0;;) {
        cbar();
        v4u t[N]; unsigned ok = 1u;
#pragma unroll
        for (int i = 0; i < N; ++i) t[i] = ld_sc1(wr, OFF_MOD + eo[i] * 4);
#pragma unroll
        for (int i = 0; i < N; ++i) { ok &= t[i].x & t[i].y & t[i].z & t[i].w; out[i] = asf(t[i] & 0xffff0000u); }
        if (__all(ok != 0u) || dead) break;
        __builtin_amdgcn_s_sleep(4);
        spin_tick(spins, dead, p.ws);
    }
}
DEVI float sum16(float v) {
    v += __int_as_float(__builtin_amdgcn_update_dpp(0, __float_as_int(v), 0x128, 0xf, 0xf, false));
    v += __int_as_float(__builtin_amdgcn_update_dpp(0, __float_as_int(v), 0x124, 0xf, 0xf, false));
    v += __int_as_float(__builtin_amdgcn_update_dpp(0, __float_as_int(v), 0x122, 0xf, 0xf, false));
    v += __int_as_float(__builtin_amdgcn_update_dpp(0, __float_as_int(v), 0x121, 0xf, 0xf, false));
    return v;
}
DEVI void ln1_segment(const Params& p, rsrc_t wr, int seg, unsigned& dead) {
    const int tid = threadIdx.x, i16 = tid & 15, r = tid >> 4;
    float xv[2][40], rstd[2];
    {
        v4u xin[2][5];
#pragma unroll
        for (int k = 0; k < 2; ++k)
#pragma unroll
            for (int c = 0; c < 5; ++c) xin[k][c] = gld16(p.x + (size_t)(seg * 32 + 16 * k + r) * DM + (i16 + 16 * c) * 8);
#pragma unroll
        for (int k = 0; k < 2; ++k) {
            float s = 0;
#pragma unroll
            for (int c = 0; c < 5; ++c)
#pragma unroll
                for (int j = 0; j < 4; ++j) { xv[k][c * 8 + 2 * j] = bflo(xin[k][c][j]); xv[k][c * 8 + 2 * j + 1] = bfhi(xin[k][c][j]); s += xv[k][c * 8 + 2 * j] + xv[k][c * 8 + 2 * j + 1]; }
            const float mean = sum16(s) * (1.f / DM);
            float q = 0;
#pragma unroll
            for (int j = 0; j < 40; ++j) { xv[k][j] -= mean; q += xv[k][j] * xv[k][j]; }
            rstd[k] = __builtin_amdgcn_rsqf(sum16(q) * (1.f / DM) + 1e-5f);
        }
    }
    unsigned eo[20]; f32x4 mv[20];
#pragma unroll
    for (int i = 0; i < 20; ++i) eo[i] = (i & 1) * DM + (i16 + 16 * (i >> 2)) * 8 + ((i >> 1) & 1) * 4;
    mod_load<20>(p, wr, eo, mv, dead);
#pragma unroll
    for (int c = 0; c < 5; ++c) {
        float sc[8];
#pragma unroll
        for (int j = 0; j < 8; ++j) sc[j] = rbf16(1.f + mv[c * 4 + (j >> 2) * 2 + 1][j & 3]);
#pragma unroll
        for (int k = 0; k < 2; ++k) {
            float hh[8];
#pragma unroll
            for (int j = 0; j < 8; ++j) hh[j] = xv[k][c * 8 + j] * rstd[k] * sc[j] + mv[c * 4 + (j >> 2) * 2][j & 3];
            const v4u o = {pk2(hh[0], hh[1]), pk2(hh[2], hh[3]), pk2(hh[4], hh[5]), pk2(hh[6], hh[7])};
            st_sc1(wr, OFF_HF + (unsigned)seg * 40960 + (i16 + 16 * c) * 512 + (16 * k + r) * 16, o);
        }
    }
    drain();
    __syncthreads();
    if (tid == 0) ctl_st(p.ws, C_H + seg * 128, 1u);
}

DEVI void role_PZ(const Params& p, lds8* L, int v, rsrc_t wr) {
    const int tid = threadIdx.x, lane = tid & 63, wave = __builtin_amdgcn_readfirstlane(tid >> 6);
    const int g = lane >> 4, c = lane & 15;
    unsigned dead = 0;
    if (v < 20) mod_block(p, L, wr, v);
    ln1_segment(p, wr, v < 20 ? 20 + v : v - 20, dead);
    mod_block(p, L, wr, 20 + v);
    const unsigned nb = get_nb(p, dead);
    const int set = v & 1, cb = v >> 1;
    const int myv = 2 * cb + (wave >> 1), myq = wave & 1;
    f32x4 gav[1];
    { const unsigned eo[1] = {(unsigned)(2 * DM + 16 * myv + 4 * g)}; mod_load<1>(p, wr, eo, gav, dead); }
    const f32x4 ga = gav[0];
    const unsigned uo = OFF_UT + (unsigned)(8 * wave) * 40960 + cb * 2048 + lane * 16;
    const unsigned yo = N_YT + (unsigned)(8 * wave + g) * 10240 + cb * 512 + c * 16;
    const unsigned pxo = OFF_PX + (unsigned)(8 * wave) * 2560 + lane * 16;
    const unsigned pmo = OFF_PX + (unsigned)(8 * wave + g) * 2560 + 2048 + c * 16;
    const unsigned fso = C_FS + (((lane >> 3) & 1) * 32 + 8 * wave + (lane & 7)) * 128;
    unsigned fu = 0u, fs = 0u;
    v4u z[20];
    v2u xr;
    const rsrc_t xrs = mkrs(p.x, 2048 * DM * 2);
    __syncthreads();
    wait_prog(p, C_FU + (8 * wave + (lane & 7)) * 128, (unsigned)set + 1u, fu, dead);
#pragma unroll
    for (int i = 0; i < 16; ++i) z[i] = ld_sc1(wr, uo + (unsigned)set * (NH * 40960) + (i >> 1) * 40960 + (i & 1) * 1024);
    xr = __builtin_amdgcn_raw_buffer_load_b64(xrs, (unsigned)((set * 32 + myq * 16 + c) * DM + 16 * myv + 4 * g) * 2, 0, 0);
    for (int s = set; s < NSEG; s += 2) {
        v4u pf[20];
        const int sn = min(s + 2, NSEG - 1);
        wait_prog(p, C_FU + (8 * wave + (lane & 7)) * 128, (unsigned)sn + 1u, fu, dead);
        wait_prog(p, fso, (unsigned)s + 1u, fs, dead);
        LSTAMP(s, 0, rtime());
        const unsigned pb = (unsigned)s * (NH * 2560);
#pragma unroll
        for (int i = 0; i < 16; ++i) pf[i] = ld_sc1(wr, ((i & 1) == 0 && (g & 1)) ? OOB : pxo + pb + (i >> 1) * 2560 + (i & 1) * 1024);
#pragma unroll
        for (int i = 0; i < 4; ++i) z[16 + i] = ld_sc1(wr, physo(yo + (unsigned)s * (NH * 10240) + (i >> 1) * 4 * 10240 + (i & 1) * 256, nb));
#pragma unroll
        for (int i = 0; i < 4; ++i) pf[16 + i] = ld_sc1(wr, pmo + pb + (i >> 1) * 4 * 2560 + (i & 1) * 256);
        __builtin_amdgcn_sched_barrier(0);
        f32x4 o[4];
        MFMA_V0(o[0], z[0], pf[0]); MFMA_V0(o[2], z[1], pf[0]); MFMA_V0(o[1], z[0], pf[1]); MFMA_V0(o[3], z[1], pf[1]);
#pragma unroll
        for (int k = 1; k < 10; ++k) { MFMA_V(o[0], z[2 * k], pf[2 * k]); MFMA_V(o[2], z[2 * k + 1], pf[2 * k]); MFMA_V(o[1], z[2 * k], pf[2 * k + 1]); MFMA_V(o[3], z[2 * k + 1], pf[2 * k + 1]); }
        asm volatile("s_nop 7" : "+v"(o[0]), "+v"(o[1]), "+v"(o[2]), "+v"(o[3]));
#pragma unroll
        for (int t = 0; t < 4; ++t) lds_wr16(L + (wave * 4 + t) * 1024 + lane * 16, asu(o[t]));
        bar();
        {
            lds8* R = L + wave * 1024 + lane * 16;
            const f32x4 r = (asf(lds_rd16(R)) + asf(lds_rd16(R + 4096))) + (asf(lds_rd16(R + 8192)) + asf(lds_rd16(R + 12288)));
            const unsigned m0 = pk2(bflo(xr.x) + ga[0] * r[0], bfhi(xr.x) + ga[1] * r[1]), m1 = pk2(bflo(xr.y) + ga[2] * r[2], bfhi(xr.y) + ga[3] * r[3]);
            st_sc1(wr, (g & 1) ? OOB : OFF_X1F + (unsigned)s * 40960 + (2 * myv + (g >> 1)) * 512 + (myq * 16 + c) * 16, sw16x2(m0, m0, m1, m1));
            const float a0 = bflo(m0), a1 = bfhi(m0), a2 = bflo(m1), a3 = bfhi(m1);
            const float s1 = xsum_g((a0 + a1) + (a2 + a3)), s2 = xsum_g((a0 * a0 + a1 * a1) + (a2 * a2 + a3 * a3));
            const v2u st = {__float_as_uint(s1), __float_as_uint(s2)};
            __builtin_amdgcn_raw_buffer_store_b64(st, wr, g == 0 ? OFF_ST + (unsigned)(s * 40 + myv) * 256 + (myq * 16 + c) * 8 : OOB, 0, 16);
        }
        drain();
        bar();
        if (tid == 0) __hip_atomic_fetch_add((GAS unsigned*)(p.ws + C_PZ + s * 128), 1u, RLX_AGENT);
        LSTAMP(s, 1, rtime());
        cbar();
#pragma unroll
        for (int i = 0; i < 16; ++i) z[i] = ld_sc1(wr, uo + (unsigned)sn * (NH * 40960) + (i >> 1) * 40960 + (i & 1) * 1024);
        xr = __builtin_amdgcn_raw_buffer_load_b64(xrs, (unsigned)((sn * 32 + myq * 16 + c) * DM + 16 * myv + 4 * g) * 2, 0, 0);
    }
    STAMP_DUMP();
}

DEVI void role_F1(const Params& p, lds8* L, int cb, rsrc_t wr) {
    const int tid = threadIdx.x, lane = tid & 63, wave = __builtin_amdgcn_readfirstlane(tid >> 6);
    const int hi = lane >> 5;
    unsigned dead = 0;
    if (cb < 24) ln1_segment(p, wr, 40 + cb, dead);
    wait_ge(p.ws, C_FK, 1u, dead);
    bf16x8 w[40];
    f32x16 cs, tb;
    {
        lds8* W = L + wave * 40960;
        const int pc = lane & 3;
        const unsigned char* src = (const unsigned char*)(pc < 2 ? p.w1 : p.w3) + (size_t)(lane >> 2) * (FF * 2) + (cb * 64 + wave * 16) * 2 + (pc & 1) * 16;
        stage64<640>(src, FF * 2, W, lane);
        lds_fence();
        const int blk = (lane >> 4) & 1, q4 = (lane & 15) >> 2, p4 = lane & 3;
        lds8* b = W + (8 * hi + q4) * 64 + (((2 * blk + (p4 & 1)) ^ (hi << 1)) * 16) + (p4 >> 1) * 8;
#pragma unroll
        for (int k = 0; k < 16; ++k) { cs[k] = 0.f; tb[k] = 0.f; }
        const v4u ones = {0x3f803f80u, 0x3f803f80u, 0x3f803f80u, 0x3f803f80u};
#pragma unroll
        for (int k4 = 0; k4 < 10; ++k4) {
            unsigned eo[16]; f32x4 mv[16];
#pragma unroll
            for (int i = 0; i < 16; ++i) eo[i] = (3 + (i & 1)) * DM + (k4 * 4 + (i >> 2)) * 16 + 8 * hi + ((i >> 1) & 1) * 4;
            mod_load<16>(p, wr, eo, mv, dead);
#pragma unroll
            for (int i = 0; i < 4; ++i) {
                const int ks = k4 * 4 + i;
                const v4u f = __builtin_bit_cast(v4u, fragA32(b, ks));
                const v4u sbv = {pk2(mv[i * 4][0], mv[i * 4][1]), pk2(mv[i * 4][2], mv[i * 4][3]), pk2(mv[i * 4 + 2][0], mv[i * 4 + 2][1]), pk2(mv[i * 4 + 2][2], mv[i * 4 + 2][3])};
                tb = mfma32(as8(f), as8(sbv), tb);
                v4u fs;
#pragma unroll
                for (int j = 0; j < 4; ++j) { const f32x4 cv = mv[i * 4 + (j >> 1) * 2 + 1]; fs[j] = pk2(bflo(f[j]) * rbf16(1.f + cv[(j & 1) * 2]), bfhi(f[j]) * rbf16(1.f + cv[(j & 1) * 2 + 1])); }
                w[ks] = as8(fs);
                cs = mfma32(w[ks], as8(ones), cs);
                asm volatile("" : "+a"(w[ks]));
            }
        }
        lds_fence();
    }
    __syncthreads();
    const unsigned char* xsrc = p.ws + OFF_X1F + wave * 10240 + lane * 16;
    v2u stv[5];
    bool have = false;
    for (int s = 0; s < NSEG; ++s) {
        if (!have) {
            wait_ge(p.ws, C_PZ + s * 128, NPZ, dead);
#pragma unroll
            for (int i = 0; i < 10; ++i) dma_piece(xsrc + (size_t)s * 40960, L + (s & 1) * 40960 + wave * 10240, i);
#pragma unroll
            for (int i = 0; i < 5; ++i) stv[i] = __builtin_amdgcn_raw_buffer_load_b64(wr, OFF_ST + (unsigned)(s * 40 + wave * 10 + i * 2) * 256 + lane * 8, 0, 16);
            __builtin_amdgcn_sched_barrier(0);
            drain();
        }
        LSTAMP(s, 0, rtime());
        float s1 = 0.f, s2 = 0.f;
#pragma unroll
        for (int i = 0; i < 5; ++i) { asm volatile("" : "+v"(stv[i])); s1 += __uint_as_float(stv[i].x); s2 += __uint_as_float(stv[i].y); }
        *(LAS v2u*)(L + 81920 + wave * 512 + lane * 8) = (v2u){__float_as_uint(s1), __float_as_uint(s2)};
        bar();
        CSTAMP(s, 2);
        const unsigned nf = ld4_sc1(wr, s + 1 < NSEG ? C_PZ + (s + 1) * 128 : OOB);
        lds8* B = L + (s & 1) * 40960 + lane * 16;
        const unsigned char* nsrc = xsrc + (size_t)(s + 1) * 40960;
        lds8* ndst = L + ((s + 1) & 1) * 40960 + wave * 10240;
        f32x16 d;
#pragma unroll
        for (int k = 0; k < 16; ++k) d[k] = 0.f;
        v4u bf[2][5];
#pragma unroll
        for (int k = 0; k < 5; ++k) bf[0][k] = lds_rd16(B + k * 1024);
        s1 = 0.f; s2 = 0.f;
#pragma unroll
        for (int i = 0; i < 8; ++i) { const v2u t = *(LAS v2u*)(L + 81920 + i * 256 + (lane & 31) * 8); s1 += __uint_as_float(t.x); s2 += __uint_as_float(t.y); }
        const float mean = s1 * (1.f / DM);
        const float rstd = __builtin_amdgcn_rsqf(fmaxf(s2 * (1.f / DM) - mean * mean, 0.f) + 1e-5f);
        __builtin_amdgcn_sched_barrier(0);
#pragma unroll
        for (int ch = 0; ch < 8; ++ch) {
            if (ch == 3) have = __builtin_amdgcn_readfirstlane(nf) >= NPZ;
#pragma unroll
            for (int k = 0; k < 5; ++k) {
                d = mfma32(w[ch * 5 + k], as8(bf[ch & 1][k]), d);
                __builtin_amdgcn_sched_barrier(0);
                if (k == 0 && ch + 1 < 8) {
#pragma unroll
                    for (int k2 = 0; k2 < 5; ++k2) bf[(ch + 1) & 1][k2] = lds_rd16(B + ((ch + 1) * 5 + k2) * 1024);
                }
                if (ch >= 3 && (k == 2 || k == 4) && have) dma_piece(nsrc, ndst, (ch - 3) * 2 + (k >> 2));
                __builtin_amdgcn_sched_barrier(0);
            }
        }
        CSTAMP(s, 5);
#pragma unroll
        for (int i = 0; i < 5; ++i) stv[i] = __builtin_amdgcn_raw_buffer_load_b64(wr, have ? OFF_ST + (unsigned)((s + 1) * 40 + wave * 10 + i * 2) * 256 + lane * 8 : OOB, 0, 16);
        __builtin_amdgcn_sched_barrier(0);
        const float mr = mean * rstd;
        float o[8];
#pragma unroll
        for (int k = 0; k < 8; ++k) {
            const float gt = rstd * d[k] + (tb[k] - mr * cs[k]), up = rstd * d[8 + k] + (tb[8 + k] - mr * cs[8 + k]);
            o[k] = gt * up * __builtin_amdgcn_rcpf(1.f + __builtin_amdgcn_exp2f(gt * -1.4426950408889634f));
        }
        const v4u ov = {pk2(o[0], o[1]), pk2(o[2], o[3]), pk2(o[4], o[5]), pk2(o[6], o[7])};
        st_sc1(wr, OFF_GF + (unsigned)((s * 2 + ((lane >> 4) & 1)) * 216 + (cb * 4 + wave) * 2 + hi) * 256 + (lane & 15) * 16, ov);
        CSTAMP(s, 6);
        asm volatile("s_waitcnt vmcnt(0)" : "+v"(stv[0]), "+v"(stv[1]), "+v"(stv[2]), "+v"(stv[3]), "+v"(stv[4]) :: "memory");
        bar();
        if (tid == 0) __hip_atomic_fetch_add((GAS unsigned*)(p.ws + C_F1 + s * 128), 1u, RLX_AGENT);
        CSTAMP(s, 7);
        LSTAMP(s, 1, rtime());
    }
    STAMP_DUMP();
}

DEVI void role_F2(const Params& p, lds8* L, int cb, int th, rsrc_t wr) {
    const int tid = threadIdx.x, lane = tid & 63, wave = __builtin_amdgcn_readfirstlane(tid >> 6);
    const int g = lane >> 4, c = lane & 15;
    unsigned dead = 0;
    wait_ge(p.ws, C_FK, 1u, dead);
    const int kb = wave < 2 ? wave * 14 : 28 + (wave - 2) * 13;
    bf16x8 w[56];
    {
        lds8* W = L + wave * 28672;
        const int q4 = (lane & 15) >> 2, p4 = lane & 3;
#pragma unroll
        for (int t2 = 0; t2 < 2; ++t2) {
            v4u v[28];
#pragma unroll
            for (int it = 0; it < 28; ++it) { const int row = min(kb * 32 + it * 16 + (lane >> 2), FF - 1); v[it] = gld16((const unsigned char*)p.w2 + (size_t)row * (DM * 2) + (cb * 64 + t2 * 32) * 2 + (lane & 3) * 16); }
            const int r = lane >> 2, pc = lane & 3;
            lds8* d = W + r * 64 + ((pc ^ (((r >> 3) & 1) << 1)) * 16);
#pragma unroll
            for (int it = 0; it < 28; ++it) lds_wr16(d + it * 1024, v[it]);
            lds_fence();
#pragma unroll
            for (int t = 0; t < 2; ++t) {
                lds8* b = W + (8 * g + q4) * 64 + (((2 * t + (p4 >> 1)) ^ ((g & 1) << 1)) * 16) + (p4 & 1) * 8;
#pragma unroll
                for (int ks = 0; ks < 14; ++ks) w[(t2 * 2 + t) * 14 + ks] = cat8(lds_tr(b + ks * 2048), lds_tr(b + ks * 2048 + 256));
            }
#pragma unroll
            for (int i = 0; i < 28; ++i) asm volatile("" : "+a"(w[t2 * 28 + i]));
            lds_fence();
        }
    }
    const int col = cb * 64 + wave * 16 + 4 * g;
    f32x4 gbv[1];
    { const unsigned eo[1] = {(unsigned)(5 * DM + col)}; mod_load<1>(p, wr, eo, gbv, dead); }
    const f32x4 gb = gbv[0];
    __syncthreads();
    for (int s = 0; s < NSEG; ++s) {
        wait_ge(p.ws, C_F1 + s * 128, 27u, dead);
        LSTAMP(s, 0, rtime());
        v4u gfr[14];
        const unsigned gbase = OFF_GF + (unsigned)((s * 2 + th) * 216 + kb * 4) * 256 + lane * 16;
#pragma unroll
        for (int ks = 0; ks < 14; ++ks) gfr[ks] = ld_sc1(wr, (ks == 13 && wave >= 2) ? OOB : gbase + ks * 1024);
        const v2u xv = __builtin_amdgcn_raw_buffer_load_b64(wr, OFF_X1F + (unsigned)s * 40960 + (col >> 3) * 512 + (th * 16 + c) * 16 + (g & 1) * 8, 0, 16);
        __builtin_amdgcn_sched_barrier(0);
        f32x4 d[4];
#pragma unroll
        for (int t = 0; t < 4; ++t) d[t] = (f32x4){0.f, 0.f, 0.f, 0.f};
#pragma unroll
        for (int ks = 0; ks < 14; ++ks)
#pragma unroll
            for (int t = 0; t < 4; ++t) d[t] = mfma16(w[t * 14 + ks], as8(gfr[ks]), d[t]);
        lds8* R = L + (s & 1) * 16384;
#pragma unroll
        for (int t = 0; t < 4; ++t) lds_wr16(R + (wave * 4 + t) * 1024 + lane * 16, asu(d[t]));
        bar();
        const f32x4 r = (asf(lds_rd16(R + wave * 1024 + lane * 16)) + asf(lds_rd16(R + (4 + wave) * 1024 + lane * 16)))
                      + (asf(lds_rd16(R + (8 + wave) * 1024 + lane * 16)) + asf(lds_rd16(R + (12 + wave) * 1024 + lane * 16)));
        const unsigned m0 = pk2(bflo(xv.x) + gb[0] * r[0], bfhi(xv.x) + gb[1] * r[1]), m1 = pk2(bflo(xv.y) + gb[2] * r[2], bfhi(xv.y) + gb[3] * r[3]);
        const v4u o = sw16x2(m0, m0, m1, m1);
        if (!(g & 1)) *(GAS v4u*)(p.out + (size_t)(s * 32 + th * 16 + c) * DM + col) = o;
        LSTAMP(s, 1, rtime());
    }
    STAMP_DUMP();
}

__global__ void __launch_bounds__(256, 1) k_main(Params p) {
    extern __shared__ __attribute__((aligned(16))) unsigned char smem[];
    lds8* L = (lds8*)smem;
    const int b = blockIdx.x, idx = (b >> 3) * 4 + (b & 3);
    const rsrc_t wr = mkrs(p.ws, WS_USED);
    const int id = idx + ((b & 4) ? 128 : 0);
    if (id < 32) role_S(p, L, id, wr);
    else if (id < 64) role_Y(p, L, id - 32, wr);
    else if (id < 104) role_V(p, L, id - 64, wr);
    else if (id < 124) role_F2(p, L, (id - 104) >> 1, id & 1, wr);
    else if (id < 127 || id >= 232) role_F1(p, L, id < 127 ? id - 124 + 24 : id - 232, wr);
    else if (id < 128) return;
    else if (id < 160) role_XQK(p, L, id - 128, wr);
    else if (id < 192) role_XVU(p, L, id - 160, wr);
    else role_PZ(p, L, id - 192, wr);
}

extern "C" void kernel_launch(void* const* d_in, const int* in_sizes, int n_in, void* d_out, int out_size, void* d_ws, size_t ws_size, hipStream_t stream) {
    Params p;
    p.x = (const u16*)d_in[0]; p.mem0 = (const u16*)d_in[1]; p.wq = (const u16*)d_in[2]; p.wk = (const u16*)d_in[3]; p.wv = (const u16*)d_in[4]; p.wo = (const u16*)d_in[5];
    p.w1 = (const u16*)d_in[8]; p.w3 = (const u16*)d_in[9]; p.w2 = (const u16*)d_in[10]; p.c = (const u16*)d_in[11]; p.wmod = (const u16*)d_in[12];
    p.ws = (unsigned char*)d_ws; p.out = (u16*)d_out;
    (void)hipFuncSetAttribute((const void*)k_main, hipFuncAttributeMaxDynamicSharedMemorySize, LDS_MAIN);
    (void)hipMemsetAsync(d_ws, 0, CTL_BYTES, stream);
    k_main<<<256, 256, LDS_MAIN, stream>>>(p);
}
```
